```python
import torch

def _softmax(x, axis=-1):
    return torch.softmax(x, dim=axis)
def _gelu(x):
    return torch.nn.functional.gelu(x, approximate="tanh")
def _silu(x):
    return torch.nn.functional.silu(x)
def _sigmoid(x):
    return torch.sigmoid(x)
def _layernorm(x, axis=-1, eps=1e-5):
    m = x.mean(dim=axis, keepdim=True)
    v = x.var(dim=axis, keepdim=True, unbiased=False)
    return (x - m) / torch.sqrt(v + eps)
def _scatter_add(tgt, idx, src, axis=0):
    return tgt.clone().index_add_(axis, idx, src)
def _segment_sum(src, seg, n):
    out = torch.zeros((n,) + src.shape[1:], dtype=src.dtype)
    return out.index_add_(0, seg, src)
def _pad(x, pw):
    flat = tuple(v for pair in reversed(pw) for v in pair)
    return torch.nn.functional.pad(x, flat)

kernel_name = 'standalone__gqa_softmax__sampling'

def setup_inputs():

    _w = (lambda a: (lambda t: t.to(torch.bfloat16) if t.is_floating_point() else t)(torch.as_tensor(a)))
    B, Hq, Hkv, Sq, Sk, D = 8, 32, 8, 32, 2048, 128
    return {
        "q": _w((lambda *s: torch.randn(*s, dtype=torch.bfloat16) * 0.8)(B, Hq, Sq, D)),
        "k": _w((lambda *s: torch.randn(*s, dtype=torch.bfloat16) * 0.8)(B, Hkv, Sk, D)),
        "v": _w((lambda *s: torch.randn(*s, dtype=torch.bfloat16) * 0.05)(B, Hkv, Sk, D)),
    }

def reference(q, k, v):
    B, Hq, Sq, D = q.shape
    Hkv = k.shape[1]
    rep = Hq // Hkv
    k = (lambda x, n, axis: torch.repeat_interleave(x, n, dim=axis))(k, rep, axis=1)
    v = (lambda x, n, axis: torch.repeat_interleave(x, n, dim=axis))(v, rep, axis=1)
    scale = D ** -0.5
    scores = torch.einsum("bhqd,bhkd->bhqk", q, k) * scale
    p = _softmax(scores, axis=-1)
    return torch.einsum("bhqk,bhkd->bhqd", p, v)
```

```cpp
#include <hip/hip_runtime.h>
#include <hip/hip_bf16.h>

typedef short bf16x8 __attribute__((ext_vector_type(8)));
typedef short s16x4 __attribute__((ext_vector_type(4)));
typedef float f32x16 __attribute__((ext_vector_type(16)));
typedef float f32x4 __attribute__((ext_vector_type(4)));
typedef float f32x2 __attribute__((ext_vector_type(2)));
typedef __bf16 bf16x2_t __attribute__((ext_vector_type(2)));
typedef unsigned u32x4 __attribute__((ext_vector_type(4)));
typedef __attribute__((address_space(3))) s16x4* lds_s16x4_p;
typedef __attribute__((address_space(3))) bf16x8* lds_bf16x8_p;
typedef __attribute__((address_space(3))) f32x4* lds_f32x4_p;
typedef __attribute__((address_space(3))) float* lds_f32_p;

constexpr int B_ = 8, HQ = 32, HKV = 8, SQ = 32, SK = 2048, D = 128;
constexpr int NW = 8, KT = 32, NTILE = SK / KT, TPW = NTILE / NW;
constexpr int SLOT = KT * D * 2;
constexpr int LDS_BYTES = NW * 2 * SLOT + NW * 32 * 2 * 4;
constexpr float SC = 0.088388347648318447f * 1.4426950408889634f;

__device__ __forceinline__ unsigned pk_bf16(float a, float b) {
  f32x2 v = {a, b};
  bf16x2_t r = __builtin_convertvector(v, bf16x2_t);
  return __builtin_bit_cast(unsigned, r);
}

__device__ __forceinline__ void dma_tile(const char* gbase, unsigned lds, unsigned o0, unsigned o1, unsigned o2, unsigned o3) {
  unsigned keep;
  const char* g2 = gbase + 4096;
  unsigned lds2 = lds + 4096;
  asm volatile(
      "s_waitcnt lgkmcnt(0)\n\t"
      "s_mov_b32 %0, m0\n\t"
      "s_mov_b32 m0, %7\n\t"
      "s_nop 0\n\t"
      "global_load_lds_dwordx4 %1, %5 offset:0\n\t"
      "global_load_lds_dwordx4 %2, %5 offset:1024\n\t"
      "global_load_lds_dwordx4 %3, %5 offset:2048\n\t"
      "global_load_lds_dwordx4 %4, %5 offset:3072\n\t"
      "s_mov_b32 m0, %8\n\t"
      "s_nop 0\n\t"
      "global_load_lds_dwordx4 %1, %6 offset:0\n\t"
      "global_load_lds_dwordx4 %2, %6 offset:1024\n\t"
      "global_load_lds_dwordx4 %3, %6 offset:2048\n\t"
      "global_load_lds_dwordx4 %4, %6 offset:3072\n\t"
      "s_mov_b32 m0, %0"
      : "=&s"(keep)
      : "v"(o0), "v"(o1), "v"(o2), "v"(o3), "s"(gbase), "s"(g2), "s"(lds), "s"(lds2)
      : "memory");
}

__global__ __launch_bounds__(64 * NW, 2) void gqa_kernel(const __hip_bfloat16* __restrict__ Qp, const __hip_bfloat16* __restrict__ Kp,
                                                          const __hip_bfloat16* __restrict__ Vp, __hip_bfloat16* __restrict__ Op) {
  extern __shared__ __attribute__((aligned(16))) char smem[];
  const int tid = threadIdx.x, lane = tid & 63;
  const int wid = __builtin_amdgcn_readfirstlane(tid >> 6);
  const int r32 = lane & 31, h = lane >> 5;
  const int bid = blockIdx.x, xcd = bid & 7, idx = bid >> 3;
  const int unit = xcd * 8 + (idx >> 2), g = idx & 3;
  const int qhead = unit * 4 + g;
  const char* Kb = (const char*)Kp + (size_t)unit * SK * D * 2;
  const char* Vb = (const char*)Vp + (size_t)unit * SK * D * 2;
  const char* Qb = (const char*)Qp + (size_t)qhead * SQ * D * 2;

  const unsigned smem_base = (unsigned)(uintptr_t)smem;
  const unsigned kslot = smem_base + wid * 2 * SLOT, vslot = kslot + SLOT;

  const int li = lane & 15, lq = lane >> 4;
  const unsigned kb0 = lq * 256 + ((li ^ lq) * 16);
  const unsigned ko0 = kb0, ko1 = kb0 ^ 64, ko2 = kb0 ^ 128, ko3 = kb0 ^ 192;
  const unsigned vo = lq * 256 + ((li ^ (lq << 2)) * 16);

  const char* kt = Kb + (size_t)wid * SLOT;
  const char* vt = Vb + (size_t)wid * SLOT;
  dma_tile(kt, kslot, ko0, ko1, ko2, ko3);
  dma_tile(vt, vslot, vo, vo, vo, vo);

  bf16x8 qf[8];
#pragma unroll
  for (int ks = 0; ks < 8; ++ks) qf[ks] = *(const bf16x8*)(Qb + r32 * 256 + ks * 32 + h * 16);
#pragma unroll
  for (int ks = 0; ks < 8; ++ks) asm volatile("" : "+v"(qf[ks]));

  const unsigned kr = kslot + r32 * 256 + ((h ^ (r32 & 15)) * 16);
  const int tq = (lane & 15) >> 2, tp = lane & 3, tblk = (lane >> 4) & 1;
  const unsigned vr = vslot + 256 * (4 * h + tq) + 32 * tblk + 8 * tp;

  f32x16 o[4];
#pragma unroll
  for (int i = 0; i < 4; ++i)
#pragma unroll
    for (int j = 0; j < 16; ++j) o[i][j] = 0.f;
  float m = -__builtin_inff(), l = 0.f;

  for (int t = 0; t < TPW; ++t) {
    asm volatile("s_waitcnt vmcnt(8)" ::: "memory");
    f32x16 s;
#pragma unroll
    for (int j = 0; j < 16; ++j) s[j] = 0.f;
#pragma unroll
    for (int ks = 0; ks < 8; ++ks) {
      bf16x8 kf = *(lds_bf16x8_p)(uintptr_t)(kr ^ (32 * ks));
      s = __builtin_amdgcn_mfma_f32_32x32x16_bf16(kf, qf[ks], s, 0, 0, 0);
    }
    kt += NW * SLOT;
    if (t + 1 < TPW) dma_tile(kt, kslot, ko0, ko1, ko2, ko3);
    float mx = s[0];
#pragma unroll
    for (int j = 1; j < 16; ++j) mx = fmaxf(mx, s[j]);
    {
      auto rr = __builtin_amdgcn_permlane32_swap(__float_as_uint(mx), __float_as_uint(mx), false, false);
      mx = fmaxf(__uint_as_float(rr[0]), __uint_as_float(rr[1]));
    }
    const float mn = fmaxf(m, mx * SC);
    const float alpha = __builtin_amdgcn_exp2f(m - mn);
    m = mn;
    float ps = 0.f;
#pragma unroll
    for (int j = 0; j < 16; ++j) {
      s[j] = __builtin_amdgcn_exp2f(fmaf(s[j], SC, -mn));
      ps += s[j];
    }
    l = l * alpha + ps;
#pragma unroll
    for (int i = 0; i < 4; ++i)
#pragma unroll
      for (int j = 0; j < 16; ++j) o[i][j] *= alpha;
    bf16x8 pf[2];
#pragma unroll
    for (int s2 = 0; s2 < 2; ++s2) {
      u32x4 w = {pk_bf16(s[8 * s2 + 0], s[8 * s2 + 1]), pk_bf16(s[8 * s2 + 2], s[8 * s2 + 3]), pk_bf16(s[8 * s2 + 4], s[8 * s2 + 5]),
                 pk_bf16(s[8 * s2 + 6], s[8 * s2 + 7])};
      pf[s2] = __builtin_bit_cast(bf16x8, w);
    }
    if (t + 1 < TPW) asm volatile("s_waitcnt vmcnt(8)" ::: "memory");
    else asm volatile("s_waitcnt vmcnt(0)" ::: "memory");
#pragma unroll
    for (int db = 0; db < 4; ++db) {
      const unsigned va = vr + 64 * (db ^ tq);
#pragma unroll
      for (int s2 = 0; s2 < 2; ++s2) {
        s16x4 lo = __builtin_amdgcn_ds_read_tr16_b64_v4i16((lds_s16x4_p)(uintptr_t)(va + 4096 * s2));
        s16x4 hi = __builtin_amdgcn_ds_read_tr16_b64_v4i16((lds_s16x4_p)(uintptr_t)(va + 4096 * s2 + 2048));
        bf16x8 vf = __builtin_shufflevector(lo, hi, 0, 1, 2, 3, 4, 5, 6, 7);
        o[db] = __builtin_amdgcn_mfma_f32_32x32x16_bf16(vf, pf[s2], o[db], 0, 0, 0);
      }
    }
    vt += NW * SLOT;
    if (t + 1 < TPW) dma_tile(vt, vslot, vo, vo, vo, vo);
  }

  {
    auto rr = __builtin_amdgcn_permlane32_swap(__float_as_uint(l), __float_as_uint(l), false, false);
    l = __uint_as_float(rr[0]) + __uint_as_float(rr[1]);
  }
  const unsigned mlb = smem_base + NW * 2 * SLOT;
  if (h == 0) {
    *(lds_f32_p)(uintptr_t)(mlb + (wid * 32 + r32) * 8) = m;
    *(lds_f32_p)(uintptr_t)(mlb + (wid * 32 + r32) * 8 + 4) = l;
  }
#pragma unroll
  for (int db = 0; db < 4; ++db)
#pragma unroll
    for (int gq = 0; gq < 4; ++gq) {
      f32x4 v = {o[db][4 * gq], o[db][4 * gq + 1], o[db][4 * gq + 2], o[db][4 * gq + 3]};
      *(lds_f32x4_p)(uintptr_t)(kslot + 512 * r32 + 128 * db + 16 * ((2 * gq + h) ^ (r32 & 7))) = v;
    }
  __syncthreads();
  {
    const int q = tid >> 4, dc = tid & 15;
    float mw[NW], lw[NW];
    float M = -__builtin_inff();
#pragma unroll
    for (int w = 0; w < NW; ++w) {
      mw[w] = *(lds_f32_p)(uintptr_t)(mlb + (w * 32 + q) * 8);
      lw[w] = *(lds_f32_p)(uintptr_t)(mlb + (w * 32 + q) * 8 + 4);
      M = fmaxf(M, mw[w]);
    }
    float L = 0.f;
    float acc[8];
#pragma unroll
    for (int i = 0; i < 8; ++i) acc[i] = 0.f;
    const unsigned c0 = (2 * dc) ^ (q & 7), c1 = (2 * dc + 1) ^ (q & 7);
#pragma unroll
    for (int w = 0; w < NW; ++w) {
      const float f = __builtin_amdgcn_exp2f(mw[w] - M);
      L += f * lw[w];
      const unsigned rb = smem_base + w * 2 * SLOT + 512 * q;
      f32x4 a = *(lds_f32x4_p)(uintptr_t)(rb + 16 * c0);
      f32x4 b = *(lds_f32x4_p)(uintptr_t)(rb + 16 * c1);
#pragma unroll
      for (int i = 0; i < 4; ++i) {
        acc[i] += f * a[i];
        acc[4 + i] += f * b[i];
      }
    }
    const float inv = 1.f / L;
    u32x4 w = {pk_bf16(acc[0] * inv, acc[1] * inv), pk_bf16(acc[2] * inv, acc[3] * inv), pk_bf16(acc[4] * inv, acc[5] * inv),
               pk_bf16(acc[6] * inv, acc[7] * inv)};
    *(u32x4*)((char*)Op + ((size_t)qhead * SQ + q) * D * 2 + dc * 16) = w;
  }
}

extern "C" void kernel_launch(void* const* d_in, const int* in_sizes, int n_in, void* d_out, int out_size, void* d_ws, size_t ws_size,
                              hipStream_t stream) {
  (void)in_sizes; (void)n_in; (void)out_size; (void)d_ws; (void)ws_size;
  (void)hipFuncSetAttribute((const void*)gqa_kernel, hipFuncAttributeMaxDynamicSharedMemorySize, LDS_BYTES);
  hipLaunchKernelGGL(gqa_kernel, dim3(B_ * HQ), dim3(64 * NW), LDS_BYTES, stream, (const __hip_bfloat16*)d_in[0],
                     (const __hip_bfloat16*)d_in[1], (const __hip_bfloat16*)d_in[2], (__hip_bfloat16*)d_out);
}
```

```cpp
#include <hip/hip_runtime.h>
#include <hip/hip_bf16.h>

typedef short bf16x8 __attribute__((ext_vector_type(8)));
typedef short s16x4 __attribute__((ext_vector_type(4)));
typedef float f32x16 __attribute__((ext_vector_type(16)));
typedef float f32x4 __attribute__((ext_vector_type(4)));
typedef float f32x2 __attribute__((ext_vector_type(2)));
typedef __bf16 bf16x2_t __attribute__((ext_vector_type(2)));
typedef unsigned u32x4 __attribute__((ext_vector_type(4)));
typedef unsigned u32x2 __attribute__((ext_vector_type(2)));
typedef __attribute__((address_space(3))) s16x4* lds_s16x4_p;
typedef __attribute__((address_space(3))) bf16x8* lds_bf16x8_p;
typedef __attribute__((address_space(3))) f32x4* lds_f32x4_p;
typedef __attribute__((address_space(3))) f32x2* lds_f32x2_p;
typedef __attribute__((address_space(1))) unsigned long long gu64;

constexpr int SQ = 32, SK = 2048, D = 128;
constexpr int NSPLIT = 4, KT = 32, KPW = SK / NSPLIT, NT = KPW / KT;
constexpr int NS = 8;
#define LA 4
static_assert(LA <= NS - 3, "lookahead");
constexpr int TILE_B = KT * D * 2;
constexpr int LDS_BYTES = NS * 2 * TILE_B;
constexpr float SC = 0.088388347648318447f * 1.4426950408889634f;
constexpr float THR = 8.f;
constexpr size_t WS_E = 0, WS_F = 4096, WS_ML = 65536, WS_O = 1 << 20;
constexpr size_t WS_END = WS_O + (size_t)64 * 4 * 4 * 16384;

__device__ __forceinline__ unsigned pk_bf16(float a, float b) {
  f32x2 v = {a, b};
  bf16x2_t r = __builtin_convertvector(v, bf16x2_t);
  return __builtin_bit_cast(unsigned, r);
}

__device__ __forceinline__ void dma8(const char* gbase, unsigned lds, unsigned o0, unsigned o1, unsigned o2, unsigned o3) {
  unsigned keep;
  const char* g2 = gbase + 4096;
  const unsigned lds2 = lds + 4096;
  asm volatile(
      "s_mov_b32 %0, m0\n\t"
      "s_mov_b32 m0, %7\n\t"
      "s_nop 0\n\t"
      "global_load_lds_dwordx4 %1, %5 offset:0\n\t"
      "global_load_lds_dwordx4 %2, %5 offset:1024\n\t"
      "global_load_lds_dwordx4 %3, %5 offset:2048\n\t"
      "global_load_lds_dwordx4 %4, %5 offset:3072\n\t"
      "s_mov_b32 m0, %8\n\t"
      "s_nop 0\n\t"
      "global_load_lds_dwordx4 %1, %6 offset:0\n\t"
      "global_load_lds_dwordx4 %2, %6 offset:1024\n\t"
      "global_load_lds_dwordx4 %3, %6 offset:2048\n\t"
      "global_load_lds_dwordx4 %4, %6 offset:3072\n\t"
      "s_mov_b32 m0, %0"
      : "=&s"(keep)
      : "v"(o0), "v"(o1), "v"(o2), "v"(o3), "s"(gbase), "s"(g2), "s"(lds), "s"(lds2)
      : "memory");
}

template <int N> __device__ __forceinline__ void wait_bar() { asm volatile("s_waitcnt vmcnt(%0)\n\ts_barrier" ::"i"(N) : "memory"); }
#define RAMP 2
template <int T, int ISS> __device__ __forceinline__ void loader_steps(const char* src, unsigned dst0, unsigned o0, unsigned o1, unsigned o2, unsigned o3) {
  if constexpr (T < NT) {
    wait_bar<8 * (ISS - T - 1)>();
    constexpr int want = T + 1 + LA < NT ? T + 1 + LA : NT;
    constexpr int nxt = ISS + RAMP < want ? ISS + RAMP : (want > ISS ? want : ISS);
#pragma unroll
    for (int t = ISS; t < nxt; ++t) dma8(src + (size_t)t * TILE_B, dst0 + (t % NS) * 2 * TILE_B, o0, o1, o2, o3);
    loader_steps<T + 1, nxt>(src, dst0, o0, o1, o2, o3);
  }
}
template <int I> __device__ __forceinline__ void tail_waits() {
  if constexpr (I >= 0) { wait_bar<8 * I>(); tail_waits<I - 1>(); }
}

__global__ __launch_bounds__(384, 2) void gqa_kernel(const __hip_bfloat16* __restrict__ Qp, const __hip_bfloat16* __restrict__ Kp,
                                                      const __hip_bfloat16* __restrict__ Vp, __hip_bfloat16* __restrict__ Op, char* __restrict__ ws) {
  extern __shared__ __attribute__((aligned(16))) char smem[];
  const int tid = threadIdx.x, lane = tid & 63;
  const int wv = __builtin_amdgcn_readfirstlane(tid >> 6);
  const int bid = blockIdx.x, xcd = bid & 7, idx = bid >> 3;
  const int unit = xcd * 8 + (idx >> 2), g = idx & 3;
  const unsigned smem_base = (unsigned)(uintptr_t)smem;

  if (__builtin_expect(wv < 2, 1)) {
    const int li = lane & 15, lq = lane >> 4;
    const unsigned sw = wv == 0 ? lq : (lq << 2);
    const unsigned o0 = lq * 256 + ((li ^ sw) * 16);
    const unsigned x = wv == 0 ? 64 : 0;
    const unsigned o1 = o0 ^ x, o2 = o0 ^ (2 * x), o3 = o0 ^ (3 * x);
    const char* src = (wv == 0 ? (const char*)Kp : (const char*)Vp) + ((size_t)unit * SK + (size_t)g * KPW) * D * 2;
    const unsigned dst0 = smem_base + wv * TILE_B;
#define P0 LA
#pragma unroll
    for (int t = 0; t < P0; ++t) dma8(src + (size_t)t * TILE_B, dst0 + t * 2 * TILE_B, o0, o1, o2, o3);
    loader_steps<0, P0>(src, dst0, o0, o1, o2, o3);
    asm volatile("s_barrier" ::: "memory");
    return;
  }
  const int wid = wv - 2;
  const int r32 = lane & 31, h = lane >> 5;
  bf16x8 qf[8];
  {
    const char* Qb = (const char*)Qp + (size_t)(unit * 4 + wid) * SQ * D * 2 + r32 * 256 + h * 16;
#pragma unroll
    for (int ks = 0; ks < 8; ++ks) {
      const u32x4 raw = *(const u32x4*)(Qb + ks * 32);
      u32x4 w;
#pragma unroll
      for (int e = 0; e < 4; ++e)
        w[e] = pk_bf16(__uint_as_float(raw[e] << 16) * SC, __uint_as_float(raw[e] & 0xffff0000u) * SC);
      qf[ks] = __builtin_bit_cast(bf16x8, w);
    }
  }
  const unsigned long long tag_pub = *(const unsigned long long*)(ws + WS_E + (unit * 4 + wid) * 8) + 1;
  const unsigned long long tag_own = *(const unsigned long long*)(ws + WS_E + (unit * 4 + g) * 8) + 1;

  const unsigned kr = smem_base + r32 * 256 + ((h ^ (r32 & 15)) * 16);
  const int tq = (lane & 15) >> 2, tp = lane & 3, tblk = (lane >> 4) & 1;
  const unsigned vr = smem_base + TILE_B + 256 * (4 * h + tq) + 32 * tblk + 8 * tp;

  f32x16 o[4];
#pragma unroll
  for (int i = 0; i < 4; ++i)
#pragma unroll
    for (int j = 0; j < 16; ++j) o[i][j] = 0.f;
  float m, l = 0.f;
  f32x16 sa, sb, negm;
  bf16x8 kf[8];
  s16x4 vlo[8], vhi[8];
  unsigned ka[8], va[4];
#pragma unroll
  for (int ks = 0; ks < 8; ++ks) ka[ks] = kr ^ (32 * ks);
#pragma unroll
  for (int db = 0; db < 4; ++db) va[db] = vr + 64 * (db ^ tq);
#define SB() __builtin_amdgcn_sched_barrier(0)
#define PIN(v) asm volatile("" : "+v"(v))
#define KREAD(ks, tile) kf[ks] = *(lds_bf16x8_p)(uintptr_t)(ka[ks] + ((tile) & 3) * 2 * TILE_B)
#define VREAD(i, tile)                                                                                                                         \
  do {                                                                                                                                         \
    vlo[i] = __builtin_amdgcn_ds_read_tr16_b64_v4i16((lds_s16x4_p)(uintptr_t)(va[(i) & 3] + 4096 * ((i) >> 2) + ((tile) & 3) * 2 * TILE_B));        \
    vhi[i] = __builtin_amdgcn_ds_read_tr16_b64_v4i16((lds_s16x4_p)(uintptr_t)(va[(i) & 3] + 4096 * ((i) >> 2) + 2048 + ((tile) & 3) * 2 * TILE_B)); \
  } while (0)
  {
    const f32x16 zero = {};
    asm volatile("s_barrier" ::: "memory");
#pragma unroll
    for (int ks = 0; ks < 8; ++ks) KREAD(ks, 0);
    sa = __builtin_amdgcn_mfma_f32_32x32x16_bf16(kf[0], qf[0], zero, 0, 0, 0);
#pragma unroll
    for (int ks = 1; ks < 8; ++ks) sa = __builtin_amdgcn_mfma_f32_32x32x16_bf16(kf[ks], qf[ks], sa, 0, 0, 0);
    SB();
    asm volatile("s_barrier" ::: "memory");
#pragma unroll
    for (int ks = 0; ks < 8; ++ks) KREAD(ks, 1);
    float mx = sa[0];
#pragma unroll
    for (int j = 1; j < 16; ++j) mx = fmaxf(mx, sa[j]);
    auto rr = __builtin_amdgcn_permlane32_swap(__float_as_uint(mx), __float_as_uint(mx), false, false);
    m = fmaxf(__uint_as_float(rr[0]), __uint_as_float(rr[1]));
#pragma unroll
    for (int j = 0; j < 16; ++j) { sa[j] -= m; negm[j] = -m; }
    SB();
  }
#define ITER(t, S, X)                                                                                              \
  {                                                                                                                \
    unsigned pw[8];                                                                                                \
    _Pragma("unroll") for (int k = 0; k < 8; ++k) {                                                                \
      if (k == 0) S = __builtin_amdgcn_mfma_f32_32x32x16_bf16(kf[0], qf[0], negm, 0, 0, 0);                        \
      else S = __builtin_amdgcn_mfma_f32_32x32x16_bf16(kf[k], qf[k], S, 0, 0, 0);                                  \
      SB();                                                                                                        \
      VREAD(k, (t) - 1);                                                                                           \
      X[2 * k] = __builtin_amdgcn_exp2f(X[2 * k]);                                                                 \
      X[2 * k + 1] = __builtin_amdgcn_exp2f(X[2 * k + 1]);                                                         \
      if (k > 0) {                                                                                                 \
        pw[k - 1] = pk_bf16(X[2 * k - 2], X[2 * k - 1]);                                                           \
        if (k & 1) { l += X[2 * k - 2]; PIN(l); }                                                                  \
      }                                                                                                            \
      SB();                                                                                                        \
    }                                                                                                              \
    pw[7] = pk_bf16(X[14], X[15]);                                                                                 \
    asm volatile("s_barrier" ::: "memory");                     \
    const bf16x8 pf0 = __builtin_bit_cast(bf16x8, u32x4{pw[0], pw[1], pw[2], pw[3]});                              \
    const bf16x8 pf1 = __builtin_bit_cast(bf16x8, u32x4{pw[4], pw[5], pw[6], pw[7]});                              \
    float mx = 0.f;                                                                                                \
    SB();                                                                                                          \
    _Pragma("unroll") for (int k = 0; k < 8; ++k) {                                                                \
      const bf16x8 vf = __builtin_shufflevector(vlo[k], vhi[k], 0, 1, 2, 3, 4, 5, 6, 7);                           \
      o[k & 3] = __builtin_amdgcn_mfma_f32_32x32x16_bf16(vf, k < 4 ? pf0 : pf1, o[k & 3], 0, 0, 0);                \
      SB();                                                                                                        \
      if ((((t) + 1) & 3) == 0) { ka[k] += ((((t) + 1) & 4) ? 8 * TILE_B : -8 * TILE_B); PIN(ka[k]); }                                               \
      if ((t) + 1 < NT) KREAD(k, (t) + 1);                                                                         \
      if ((((t)) & 3) == 0 && k < 4) { va[k] += (((t) & 4) ? 8 * TILE_B : -8 * TILE_B); PIN(va[k]); }                                          \
                                             \
      l += X[2 * k + 1];                                                                                           \
      if (k & 1) l += X[2 * k];                                                                                    \
      PIN(l);                                                                                                      \
      if (k == 2) { PIN(S); mx = fmaxf(fmaxf(S[0], S[1]), S[2]); mx = fmaxf(fmaxf(mx, S[3]), S[4]); PIN(mx); }     \
      if (k == 3) { mx = fmaxf(fmaxf(mx, S[5]), S[6]); mx = fmaxf(fmaxf(mx, S[7]), S[8]); PIN(mx); }               \
      if (k == 4) { mx = fmaxf(fmaxf(mx, S[9]), S[10]); mx = fmaxf(fmaxf(mx, S[11]), S[12]); PIN(mx); }            \
      if (k == 5) { mx = fmaxf(fmaxf(mx, S[13]), S[14]); mx = fmaxf(mx, S[15]); PIN(mx); }                         \
      if (k == 6) {                                                                                                \
        auto rr = __builtin_amdgcn_permlane32_swap(__float_as_uint(mx), __float_as_uint(mx), false, false);        \
        mx = fmaxf(__uint_as_float(rr[0]), __uint_as_float(rr[1]));                                                \
      }                                                                                                            \
      SB();                                                                                                        \
    }                                                                                                              \
    if (RESCALE_COND(mx)) {   \
      const float d = fmaxf(mx, 0.f);                                                                              \
      const float alpha = __builtin_amdgcn_exp2f(-d);                                                              \
      m += d;                                                                                                      \
      l *= alpha;                                                                                                  \
      _Pragma("unroll") for (int j = 0; j < 16; ++j) { S[j] -= d; negm[j] = -m; }                                  \
      _Pragma("unroll") for (int i = 0; i < 4; ++i) _Pragma("unroll") for (int j = 0; j < 16; ++j) o[i][j] *= alpha; \
    }                                                                                                              \
    SB();                                                                                                          \
  }
#define RESCALE_COND(mx) (__any(!(mx <= THR)))
  ITER(1, sb, sa) ITER(2, sa, sb) ITER(3, sb, sa) ITER(4, sa, sb) ITER(5, sb, sa) ITER(6, sa, sb) ITER(7, sb, sa) ITER(8, sa, sb)
  ITER(9, sb, sa) ITER(10, sa, sb) ITER(11, sb, sa) ITER(12, sa, sb) ITER(13, sb, sa) ITER(14, sa, sb) ITER(15, sb, sa)
  {
#pragma unroll
    for (int k = 0; k < 8; ++k) VREAD(k, NT - 1);
#pragma unroll
    for (int j = 0; j < 16; ++j) sb[j] = __builtin_amdgcn_exp2f(sb[j]);
    const bf16x8 pf0 = __builtin_bit_cast(bf16x8, u32x4{pk_bf16(sb[0], sb[1]), pk_bf16(sb[2], sb[3]), pk_bf16(sb[4], sb[5]), pk_bf16(sb[6], sb[7])});
    const bf16x8 pf1 = __builtin_bit_cast(bf16x8, u32x4{pk_bf16(sb[8], sb[9]), pk_bf16(sb[10], sb[11]), pk_bf16(sb[12], sb[13]), pk_bf16(sb[14], sb[15])});
#pragma unroll
    for (int k = 0; k < 8; ++k) {
      const bf16x8 vf = __builtin_shufflevector(vlo[k], vhi[k], 0, 1, 2, 3, 4, 5, 6, 7);
      o[k & 3] = __builtin_amdgcn_mfma_f32_32x32x16_bf16(vf, k < 4 ? pf0 : pf1, o[k & 3], 0, 0, 0);
    }
#pragma unroll
    for (int j = 0; j < 16; ++j) l += sb[j];
  }

  {
    auto rr = __builtin_amdgcn_permlane32_swap(__float_as_uint(l), __float_as_uint(l), false, false);
    l = __uint_as_float(rr[0]) + __uint_as_float(rr[1]);
  }
  const auto rsrc = __builtin_amdgcn_make_buffer_rsrc((void*)ws, 0, (int)WS_END, 0x00020000);
  const unsigned xo = smem_base;
  const unsigned xml = smem_base + 16384;
  if (wid != g) {
    const int slab = (unit * 4 + wid) * 4 + g;
    const unsigned ob = (unsigned)(WS_O + (size_t)slab * 16384) + lane * 16;
#pragma unroll
    for (int db = 0; db < 4; ++db)
#pragma unroll
      for (int gq = 0; gq < 4; ++gq) {
        u32x4 v = {__float_as_uint(o[db][4 * gq]), __float_as_uint(o[db][4 * gq + 1]), __float_as_uint(o[db][4 * gq + 2]), __float_as_uint(o[db][4 * gq + 3])};
        __builtin_amdgcn_raw_buffer_store_b128(v, rsrc, ob + (db * 4 + gq) * 1024, 0, 16);
      }
    if (h == 0) {
      u32x2 v = {__float_as_uint(m), __float_as_uint(l)};
      __builtin_amdgcn_raw_buffer_store_b64(v, rsrc, (unsigned)(WS_ML + slab * 256) + r32 * 8, 0, 16);
    }
    asm volatile("s_waitcnt vmcnt(0)" ::: "memory");
    if (lane == 0) __hip_atomic_store((gu64*)(ws + WS_F + slab * 8), tag_pub, __ATOMIC_RELAXED, __HIP_MEMORY_SCOPE_AGENT);
  } else {
#pragma unroll
    for (int db = 0; db < 4; ++db)
#pragma unroll
      for (int gq = 0; gq < 4; ++gq) {
        f32x4 v = {o[db][4 * gq], o[db][4 * gq + 1], o[db][4 * gq + 2], o[db][4 * gq + 3]};
        *(lds_f32x4_p)(uintptr_t)(xo + (db * 4 + gq) * 1024 + lane * 16) = v;
      }
    if (h == 0) {
      f32x2 v = {m, l};
      *(lds_f32x2_p)(uintptr_t)(xml + r32 * 8) = v;
    }
  }
  __syncthreads();
  {
    const gu64* fp = (const gu64*)(ws + WS_F + (size_t)((unit * 4 + g) * 4) * 8);
    const int sl = lane & 3;
#pragma unroll 1
    for (unsigned spins = 0; spins < 20000000u; ++spins) {
      const unsigned long long v = __hip_atomic_load(fp + sl, __ATOMIC_RELAXED, __HIP_MEMORY_SCOPE_AGENT);
      const bool ok = (sl == g) || (v == tag_own);
      if (__all(ok)) break;
      __builtin_amdgcn_s_sleep(1);
    }
    __builtin_amdgcn_fence(__ATOMIC_ACQUIRE, "wavefront");
  }
  {
    f32x2 ml0 = *(lds_f32x2_p)(uintptr_t)(xml + r32 * 8);
    u32x4 ro[3][4];
    u32x2 rml[3];
#pragma unroll
    for (int i = 0; i < 3; ++i) {
      const int sg = (g + 1 + i) & 3;
      const int slab = (unit * 4 + g) * 4 + sg;
      rml[i] = __builtin_amdgcn_raw_buffer_load_b64(rsrc, (unsigned)(WS_ML + slab * 256) + r32 * 8, 0, 16);
#pragma unroll
      for (int gq = 0; gq < 4; ++gq)
        ro[i][gq] = __builtin_amdgcn_raw_buffer_load_b128(rsrc, (unsigned)(WS_O + (size_t)slab * 16384) + lane * 16 + (wid * 4 + gq) * 1024, 0, 16);
    }
    f32x4 own[4];
#pragma unroll
    for (int gq = 0; gq < 4; ++gq) own[gq] = *(lds_f32x4_p)(uintptr_t)(xo + (wid * 4 + gq) * 1024 + lane * 16);
    float M = ml0[0];
#pragma unroll
    for (int i = 0; i < 3; ++i) M = fmaxf(M, __uint_as_float(rml[i][0]));
    const float f0 = __builtin_amdgcn_exp2f(ml0[0] - M);
    float L = f0 * ml0[1];
    float f[3];
#pragma unroll
    for (int i = 0; i < 3; ++i) {
      f[i] = __builtin_amdgcn_exp2f(__uint_as_float(rml[i][0]) - M);
      L += f[i] * __uint_as_float(rml[i][1]);
    }
    const float inv = 1.f / L;
    char* orow = (char*)Op + ((size_t)(unit * 4 + g) * SQ + r32) * D * 2 + wid * 64 + h * 8;
#pragma unroll
    for (int gq = 0; gq < 4; ++gq) {
      float a[4];
#pragma unroll
      for (int e = 0; e < 4; ++e) {
        float x = f0 * own[gq][e];
#pragma unroll
        for (int i = 0; i < 3; ++i) x += f[i] * __uint_as_float(ro[i][gq][e]);
        a[e] = x * inv;
      }
      u32x2 w = {pk_bf16(a[0], a[1]), pk_bf16(a[2], a[3])};
      *(u32x2*)(orow + gq * 16) = w;
    }
  }
  __syncthreads();
  if (tid == 128) *(unsigned long long*)(ws + WS_E + (unit * 4 + g) * 8) = tag_own;
}

extern "C" void kernel_launch(void* const* d_in, const int* in_sizes, int n_in, void* d_out, int out_size, void* d_ws, size_t ws_size,
                              hipStream_t stream) {
  (void)in_sizes; (void)n_in; (void)out_size; (void)ws_size;
  (void)hipFuncSetAttribute((const void*)gqa_kernel, hipFuncAttributeMaxDynamicSharedMemorySize, LDS_BYTES);
  hipLaunchKernelGGL(gqa_kernel, dim3(256), dim3(384), LDS_BYTES, stream, (const __hip_bfloat16*)d_in[0], (const __hip_bfloat16*)d_in[1],
                     (const __hip_bfloat16*)d_in[2], (__hip_bfloat16*)d_out, (char*)d_ws);
}
```

```cpp
#define PROBE 3
#define PROBE_BID 100
#include <hip/hip_runtime.h>
#include <hip/hip_bf16.h>

typedef short bf16x8 __attribute__((ext_vector_type(8)));
typedef short s16x4 __attribute__((ext_vector_type(4)));
typedef float f32x16 __attribute__((ext_vector_type(16)));
typedef float f32x4 __attribute__((ext_vector_type(4)));
typedef float f32x2 __attribute__((ext_vector_type(2)));
typedef __bf16 bf16x2_t __attribute__((ext_vector_type(2)));
typedef unsigned u32x4 __attribute__((ext_vector_type(4)));
typedef unsigned u32x2 __attribute__((ext_vector_type(2)));
typedef __attribute__((address_space(3))) s16x4* lds_s16x4_p;
typedef __attribute__((address_space(3))) bf16x8* lds_bf16x8_p;
typedef __attribute__((address_space(3))) f32x4* lds_f32x4_p;
typedef __attribute__((address_space(3))) f32x2* lds_f32x2_p;
typedef __attribute__((address_space(1))) unsigned long long gu64;

constexpr int SQ = 32, SK = 2048, D = 128;
constexpr int NSPLIT = 4, KT = 32, KPW = SK / NSPLIT, NT = KPW / KT;
constexpr int NS = 8;
#define LA 4
static_assert(LA <= NS - 3, "lookahead");
constexpr int TILE_B = KT * D * 2;
constexpr int LDS_BYTES = NS * 2 * TILE_B;
constexpr float SC = 0.088388347648318447f * 1.4426950408889634f;
constexpr float THR = 8.f;
constexpr size_t WS_E = 0, WS_F = 4096, WS_ML = 65536, WS_O = 1 << 20;
constexpr size_t WS_END = WS_O + (size_t)64 * 4 * 4 * 16384;

__device__ __forceinline__ unsigned pk_bf16(float a, float b) {
  f32x2 v = {a, b};
  bf16x2_t r = __builtin_convertvector(v, bf16x2_t);
  return __builtin_bit_cast(unsigned, r);
}

__device__ __forceinline__ void dma8(const char* gbase, unsigned lds, unsigned o0, unsigned o1, unsigned o2, unsigned o3) {
  unsigned keep;
  const char* g2 = gbase + 4096;
  const unsigned lds2 = lds + 4096;
  asm volatile(
      "s_mov_b32 %0, m0\n\t"
      "s_mov_b32 m0, %7\n\t"
      "s_nop 0\n\t"
      "global_load_lds_dwordx4 %1, %5 offset:0\n\t"
      "global_load_lds_dwordx4 %2, %5 offset:1024\n\t"
      "global_load_lds_dwordx4 %3, %5 offset:2048\n\t"
      "global_load_lds_dwordx4 %4, %5 offset:3072\n\t"
      "s_mov_b32 m0, %8\n\t"
      "s_nop 0\n\t"
      "global_load_lds_dwordx4 %1, %6 offset:0\n\t"
      "global_load_lds_dwordx4 %2, %6 offset:1024\n\t"
      "global_load_lds_dwordx4 %3, %6 offset:2048\n\t"
      "global_load_lds_dwordx4 %4, %6 offset:3072\n\t"
      "s_mov_b32 m0, %0"
      : "=&s"(keep)
      : "v"(o0), "v"(o1), "v"(o2), "v"(o3), "s"(gbase), "s"(g2), "s"(lds), "s"(lds2)
      : "memory");
}

template <int N> __device__ __forceinline__ void wait_bar() { asm volatile("s_waitcnt vmcnt(%0)\n\ts_barrier" ::"i"(N) : "memory"); }
#define RAMP 2
template <int T, int ISS> __device__ __forceinline__ void loader_steps(const char* src, unsigned dst0, unsigned o0, unsigned o1, unsigned o2, unsigned o3) {
  if constexpr (T < NT) {
    wait_bar<8 * (ISS - T - 1)>();
    constexpr int want = T + 1 + LA < NT ? T + 1 + LA : NT;
    constexpr int nxt = ISS + RAMP < want ? ISS + RAMP : (want > ISS ? want : ISS);
#pragma unroll
    for (int t = ISS; t < nxt; ++t) dma8(src + (size_t)t * TILE_B, dst0 + (t % NS) * 2 * TILE_B, o0, o1, o2, o3);
    loader_steps<T + 1, nxt>(src, dst0, o0, o1, o2, o3);
  }
}
template <int I> __device__ __forceinline__ void tail_waits() {
  if constexpr (I >= 0) { wait_bar<8 * I>(); tail_waits<I - 1>(); }
}

__global__ __launch_bounds__(384, 2) void gqa_kernel(const __hip_bfloat16* __restrict__ Qp, const __hip_bfloat16* __restrict__ Kp,
                                                      const __hip_bfloat16* __restrict__ Vp, __hip_bfloat16* __restrict__ Op, char* __restrict__ ws) {
  extern __shared__ __attribute__((aligned(16))) char smem[];
  const int tid = threadIdx.x, lane = tid & 63;
  const int wv = __builtin_amdgcn_readfirstlane(tid >> 6);
  const int bid = blockIdx.x, xcd = bid & 7, idx = bid >> 3;
  const int unit = xcd * 8 + (idx >> 2), g = idx & 3;
  const unsigned smem_base = (unsigned)(uintptr_t)smem;
  unsigned long long st0 = __builtin_amdgcn_s_memtime(), rt0 = __builtin_amdgcn_s_memrealtime();

  if (__builtin_expect(wv < 2, 1)) {
    const int li = lane & 15, lq = lane >> 4;
    const unsigned sw = wv == 0 ? lq : (lq << 2);
    const unsigned o0 = lq * 256 + ((li ^ sw) * 16);
    const unsigned x = wv == 0 ? 64 : 0;
    const unsigned o1 = o0 ^ x, o2 = o0 ^ (2 * x), o3 = o0 ^ (3 * x);
    const char* src = (wv == 0 ? (const char*)Kp : (const char*)Vp) + ((size_t)unit * SK + (size_t)g * KPW) * D * 2;
    const unsigned dst0 = smem_base + wv * TILE_B;
#define P0 LA
#pragma unroll
    for (int t = 0; t < P0; ++t) dma8(src + (size_t)t * TILE_B, dst0 + t * 2 * TILE_B, o0, o1, o2, o3);
    loader_steps<0, P0>(src, dst0, o0, o1, o2, o3);
    asm volatile("s_barrier" ::: "memory");
    return;
  }
  const int wid = wv - 2;
  const int r32 = lane & 31, h = lane >> 5;
  bf16x8 qf[8];
  {
    const char* Qb = (const char*)Qp + (size_t)(unit * 4 + wid) * SQ * D * 2 + r32 * 256 + h * 16;
#pragma unroll
    for (int ks = 0; ks < 8; ++ks) {
      const u32x4 raw = *(const u32x4*)(Qb + ks * 32);
      u32x4 w;
#pragma unroll
      for (int e = 0; e < 4; ++e)
        w[e] = pk_bf16(__uint_as_float(raw[e] << 16) * SC, __uint_as_float(raw[e] & 0xffff0000u) * SC);
      qf[ks] = __builtin_bit_cast(bf16x8, w);
    }
  }
  const unsigned long long tag_pub = *(const unsigned long long*)(ws + WS_E + (unit * 4 + wid) * 8) + 1;
  const unsigned long long tag_own = *(const unsigned long long*)(ws + WS_E + (unit * 4 + g) * 8) + 1;

  const unsigned kr = smem_base + r32 * 256 + ((h ^ (r32 & 15)) * 16);
  const int tq = (lane & 15) >> 2, tp = lane & 3, tblk = (lane >> 4) & 1;
  const unsigned vr = smem_base + TILE_B + 256 * (4 * h + tq) + 32 * tblk + 8 * tp;

  f32x16 o[4];
#pragma unroll
  for (int i = 0; i < 4; ++i)
#pragma unroll
    for (int j = 0; j < 16; ++j) o[i][j] = 0.f;
  float m, l = 0.f;
  f32x16 sa, sb, negm;
  bf16x8 kf[8];
  s16x4 vlo[8], vhi[8];
  unsigned ka[8], va[4];
#pragma unroll
  for (int ks = 0; ks < 8; ++ks) ka[ks] = kr ^ (32 * ks);
#pragma unroll
  for (int db = 0; db < 4; ++db) va[db] = vr + 64 * (db ^ tq);
#define SB() __builtin_amdgcn_sched_barrier(0)
#define PIN(v) asm volatile("" : "+v"(v))
#define KREAD(ks, tile) kf[ks] = *(lds_bf16x8_p)(uintptr_t)(ka[ks] + ((tile) & 3) * 2 * TILE_B)
#define VREAD(i, tile)                                                                                                                         \
  do {                                                                                                                                         \
    vlo[i] = __builtin_amdgcn_ds_read_tr16_b64_v4i16((lds_s16x4_p)(uintptr_t)(va[(i) & 3] + 4096 * ((i) >> 2) + ((tile) & 3) * 2 * TILE_B));        \
    vhi[i] = __builtin_amdgcn_ds_read_tr16_b64_v4i16((lds_s16x4_p)(uintptr_t)(va[(i) & 3] + 4096 * ((i) >> 2) + 2048 + ((tile) & 3) * 2 * TILE_B)); \
  } while (0)
  {
    const f32x16 zero = {};
    asm volatile("s_barrier" ::: "memory");
#pragma unroll
    for (int ks = 0; ks < 8; ++ks) KREAD(ks, 0);
    sa = __builtin_amdgcn_mfma_f32_32x32x16_bf16(kf[0], qf[0], zero, 0, 0, 0);
#pragma unroll
    for (int ks = 1; ks < 8; ++ks) sa = __builtin_amdgcn_mfma_f32_32x32x16_bf16(kf[ks], qf[ks], sa, 0, 0, 0);
    SB();
    asm volatile("s_barrier" ::: "memory");
#pragma unroll
    for (int ks = 0; ks < 8; ++ks) KREAD(ks, 1);
    float mx = sa[0];
#pragma unroll
    for (int j = 1; j < 16; ++j) mx = fmaxf(mx, sa[j]);
    auto rr = __builtin_amdgcn_permlane32_swap(__float_as_uint(mx), __float_as_uint(mx), false, false);
    m = fmaxf(__uint_as_float(rr[0]), __uint_as_float(rr[1]));
#pragma unroll
    for (int j = 0; j < 16; ++j) { sa[j] -= m; negm[j] = -m; }
    SB();
  }
#define ITER(t, S, X)                                                                                              \
  {                                                                                                                \
    unsigned pw[8];                                                                                                \
    _Pragma("unroll") for (int k = 0; k < 8; ++k) {                                                                \
      if (k == 0) S = __builtin_amdgcn_mfma_f32_32x32x16_bf16(kf[0], qf[0], negm, 0, 0, 0);                        \
      else S = __builtin_amdgcn_mfma_f32_32x32x16_bf16(kf[k], qf[k], S, 0, 0, 0);                                  \
      SB();                                                                                                        \
      VREAD(k, (t) - 1);                                                                                           \
      X[2 * k] = __builtin_amdgcn_exp2f(X[2 * k]);                                                                 \
      X[2 * k + 1] = __builtin_amdgcn_exp2f(X[2 * k + 1]);                                                         \
      if (k > 0) {                                                                                                 \
        pw[k - 1] = pk_bf16(X[2 * k - 2], X[2 * k - 1]);                                                           \
        if (k & 1) { l += X[2 * k - 2]; PIN(l); }                                                                  \
      }                                                                                                            \
      SB();                                                                                                        \
    }                                                                                                              \
    pw[7] = pk_bf16(X[14], X[15]);                                                                                 \
    asm volatile("s_barrier" ::: "memory");                     \
    const bf16x8 pf0 = __builtin_bit_cast(bf16x8, u32x4{pw[0], pw[1], pw[2], pw[3]});                              \
    const bf16x8 pf1 = __builtin_bit_cast(bf16x8, u32x4{pw[4], pw[5], pw[6], pw[7]});                              \
    float mx = 0.f;                                                                                                \
    SB();                                                                                                          \
    _Pragma("unroll") for (int k = 0; k < 8; ++k) {                                                                \
      const bf16x8 vf = __builtin_shufflevector(vlo[k], vhi[k], 0, 1, 2, 3, 4, 5, 6, 7);                           \
      o[k & 3] = __builtin_amdgcn_mfma_f32_32x32x16_bf16(vf, k < 4 ? pf0 : pf1, o[k & 3], 0, 0, 0);                \
      SB();                                                                                                        \
      if ((((t) + 1) & 3) == 0) { ka[k] += ((((t) + 1) & 4) ? 8 * TILE_B : -8 * TILE_B); PIN(ka[k]); }                                               \
      if ((t) + 1 < NT) KREAD(k, (t) + 1);                                                                         \
      if ((((t)) & 3) == 0 && k < 4) { va[k] += (((t) & 4) ? 8 * TILE_B : -8 * TILE_B); PIN(va[k]); }                                          \
                                             \
      l += X[2 * k + 1];                                                                                           \
      if (k & 1) l += X[2 * k];                                                                                    \
      PIN(l);                                                                                                      \
      if (k == 2) { PIN(S); mx = fmaxf(fmaxf(S[0], S[1]), S[2]); mx = fmaxf(fmaxf(mx, S[3]), S[4]); PIN(mx); }     \
      if (k == 3) { mx = fmaxf(fmaxf(mx, S[5]), S[6]); mx = fmaxf(fmaxf(mx, S[7]), S[8]); PIN(mx); }               \
      if (k == 4) { mx = fmaxf(fmaxf(mx, S[9]), S[10]); mx = fmaxf(fmaxf(mx, S[11]), S[12]); PIN(mx); }            \
      if (k == 5) { mx = fmaxf(fmaxf(mx, S[13]), S[14]); mx = fmaxf(mx, S[15]); PIN(mx); }                         \
      if (k == 6) {                                                                                                \
        auto rr = __builtin_amdgcn_permlane32_swap(__float_as_uint(mx), __float_as_uint(mx), false, false);        \
        mx = fmaxf(__uint_as_float(rr[0]), __uint_as_float(rr[1]));                                                \
      }                                                                                                            \
      SB();                                                                                                        \
    }                                                                                                              \
    if (RESCALE_COND(mx)) {   \
      const float d = fmaxf(mx, 0.f);                                                                              \
      const float alpha = __builtin_amdgcn_exp2f(-d);                                                              \
      m += d;                                                                                                      \
      l *= alpha;                                                                                                  \
      _Pragma("unroll") for (int j = 0; j < 16; ++j) { S[j] -= d; negm[j] = -m; }                                  \
      _Pragma("unroll") for (int i = 0; i < 4; ++i) _Pragma("unroll") for (int j = 0; j < 16; ++j) o[i][j] *= alpha; \
    }                                                                                                              \
    SB();                                                                                                          \
  }
#define RESCALE_COND(mx) (__any(!(mx <= THR)))
  SB(); unsigned long long ra = __builtin_amdgcn_s_memrealtime(); __builtin_amdgcn_s_waitcnt(0xC07F); SB();
  ITER(1, sb, sa) ITER(2, sa, sb) ITER(3, sb, sa) ITER(4, sa, sb) ITER(5, sb, sa) ITER(6, sa, sb) ITER(7, sb, sa) ITER(8, sa, sb)
  ITER(9, sb, sa) ITER(10, sa, sb) ITER(11, sb, sa) ITER(12, sa, sb) ITER(13, sb, sa) ITER(14, sa, sb) ITER(15, sb, sa)
  {
#pragma unroll
    for (int k = 0; k < 8; ++k) VREAD(k, NT - 1);
#pragma unroll
    for (int j = 0; j < 16; ++j) sb[j] = __builtin_amdgcn_exp2f(sb[j]);
    const bf16x8 pf0 = __builtin_bit_cast(bf16x8, u32x4{pk_bf16(sb[0], sb[1]), pk_bf16(sb[2], sb[3]), pk_bf16(sb[4], sb[5]), pk_bf16(sb[6], sb[7])});
    const bf16x8 pf1 = __builtin_bit_cast(bf16x8, u32x4{pk_bf16(sb[8], sb[9]), pk_bf16(sb[10], sb[11]), pk_bf16(sb[12], sb[13]), pk_bf16(sb[14], sb[15])});
#pragma unroll
    for (int k = 0; k < 8; ++k) {
      const bf16x8 vf = __builtin_shufflevector(vlo[k], vhi[k], 0, 1, 2, 3, 4, 5, 6, 7);
      o[k & 3] = __builtin_amdgcn_mfma_f32_32x32x16_bf16(vf, k < 4 ? pf0 : pf1, o[k & 3], 0, 0, 0);
    }
#pragma unroll
    for (int j = 0; j < 16; ++j) l += sb[j];
  }

  asm volatile("" :: "v"(o[0][0]), "v"(o[1][0]), "v"(o[2][0]), "v"(o[3][0]));
  unsigned long long rb = __builtin_amdgcn_s_memrealtime();
  {
    auto rr = __builtin_amdgcn_permlane32_swap(__float_as_uint(l), __float_as_uint(l), false, false);
    l = __uint_as_float(rr[0]) + __uint_as_float(rr[1]);
  }
  const auto rsrc = __builtin_amdgcn_make_buffer_rsrc((void*)ws, 0, (int)WS_END, 0x00020000);
  const unsigned xo = smem_base;
  const unsigned xml = smem_base + 16384;
  if (wid != g) {
    const int slab = (unit * 4 + wid) * 4 + g;
    const unsigned ob = (unsigned)(WS_O + (size_t)slab * 16384) + lane * 16;
#pragma unroll
    for (int db = 0; db < 4; ++db)
#pragma unroll
      for (int gq = 0; gq < 4; ++gq) {
        u32x4 v = {__float_as_uint(o[db][4 * gq]), __float_as_uint(o[db][4 * gq + 1]), __float_as_uint(o[db][4 * gq + 2]), __float_as_uint(o[db][4 * gq + 3])};
        __builtin_amdgcn_raw_buffer_store_b128(v, rsrc, ob + (db * 4 + gq) * 1024, 0, 16);
      }
    if (h == 0) {
      u32x2 v = {__float_as_uint(m), __float_as_uint(l)};
      __builtin_amdgcn_raw_buffer_store_b64(v, rsrc, (unsigned)(WS_ML + slab * 256) + r32 * 8, 0, 16);
    }
    asm volatile("s_waitcnt vmcnt(0)" ::: "memory");
    if (lane == 0) __hip_atomic_store((gu64*)(ws + WS_F + slab * 8), tag_pub, __ATOMIC_RELAXED, __HIP_MEMORY_SCOPE_AGENT);
  } else {
#pragma unroll
    for (int db = 0; db < 4; ++db)
#pragma unroll
      for (int gq = 0; gq < 4; ++gq) {
        f32x4 v = {o[db][4 * gq], o[db][4 * gq + 1], o[db][4 * gq + 2], o[db][4 * gq + 3]};
        *(lds_f32x4_p)(uintptr_t)(xo + (db * 4 + gq) * 1024 + lane * 16) = v;
      }
    if (h == 0) {
      f32x2 v = {m, l};
      *(lds_f32x2_p)(uintptr_t)(xml + r32 * 8) = v;
    }
  }
  __syncthreads();
  {
    const gu64* fp = (const gu64*)(ws + WS_F + (size_t)((unit * 4 + g) * 4) * 8);
    const int sl = lane & 3;
#pragma unroll 1
    for (unsigned spins = 0; spins < 20000000u; ++spins) {
      const unsigned long long v = __hip_atomic_load(fp + sl, __ATOMIC_RELAXED, __HIP_MEMORY_SCOPE_AGENT);
      const bool ok = (sl == g) || (v == tag_own);
      if (__all(ok)) break;
      __builtin_amdgcn_s_sleep(1);
    }
    __builtin_amdgcn_fence(__ATOMIC_ACQUIRE, "wavefront");
  }
  {
    f32x2 ml0 = *(lds_f32x2_p)(uintptr_t)(xml + r32 * 8);
    u32x4 ro[3][4];
    u32x2 rml[3];
#pragma unroll
    for (int i = 0; i < 3; ++i) {
      const int sg = (g + 1 + i) & 3;
      const int slab = (unit * 4 + g) * 4 + sg;
      rml[i] = __builtin_amdgcn_raw_buffer_load_b64(rsrc, (unsigned)(WS_ML + slab * 256) + r32 * 8, 0, 16);
#pragma unroll
      for (int gq = 0; gq < 4; ++gq)
        ro[i][gq] = __builtin_amdgcn_raw_buffer_load_b128(rsrc, (unsigned)(WS_O + (size_t)slab * 16384) + lane * 16 + (wid * 4 + gq) * 1024, 0, 16);
    }
    f32x4 own[4];
#pragma unroll
    for (int gq = 0; gq < 4; ++gq) own[gq] = *(lds_f32x4_p)(uintptr_t)(xo + (wid * 4 + gq) * 1024 + lane * 16);
    float M = ml0[0];
#pragma unroll
    for (int i = 0; i < 3; ++i) M = fmaxf(M, __uint_as_float(rml[i][0]));
    const float f0 = __builtin_amdgcn_exp2f(ml0[0] - M);
    float L = f0 * ml0[1];
    float f[3];
#pragma unroll
    for (int i = 0; i < 3; ++i) {
      f[i] = __builtin_amdgcn_exp2f(__uint_as_float(rml[i][0]) - M);
      L += f[i] * __uint_as_float(rml[i][1]);
    }
    const float inv = 1.f / L;
    char* orow = (char*)Op + ((size_t)(unit * 4 + g) * SQ + r32) * D * 2 + wid * 64 + h * 8;
#pragma unroll
    for (int gq = 0; gq < 4; ++gq) {
      float a[4];
#pragma unroll
      for (int e = 0; e < 4; ++e) {
        float x = f0 * own[gq][e];
#pragma unroll
        for (int i = 0; i < 3; ++i) x += f[i] * __uint_as_float(ro[i][gq][e]);
        a[e] = x * inv;
      }
      u32x2 w = {pk_bf16(a[0], a[1]), pk_bf16(a[2], a[3])};
      *(u32x2*)(orow + gq * 16) = w;
    }
  }
  {
    unsigned long long rc = __builtin_amdgcn_s_memrealtime(), sc = __builtin_amdgcn_s_memtime();
    if (bid == PROBE_BID && wid == 0) {
      unsigned long long a = PROBE == 1 || PROBE == 2 ? (unsigned long long)Kp : PROBE == 3 || PROBE == 4 ? (unsigned long long)Vp : (unsigned long long)ws;
      unsigned long long d = (PROBE & 1) ? ((a >> 4) & 255) * 20 : ((a >> 12) & 511) * 10;
      while (__builtin_amdgcn_s_memrealtime() < rc + d) __builtin_amdgcn_s_sleep(2);
    }
  }
  __syncthreads();
  if (tid == 128) *(unsigned long long*)(ws + WS_E + (unit * 4 + g) * 8) = tag_own;
}

extern "C" void kernel_launch(void* const* d_in, const int* in_sizes, int n_in, void* d_out, int out_size, void* d_ws, size_t ws_size,
                              hipStream_t stream) {
  (void)in_sizes; (void)n_in; (void)out_size; (void)ws_size;
  (void)hipFuncSetAttribute((const void*)gqa_kernel, hipFuncAttributeMaxDynamicSharedMemorySize, LDS_BYTES);
  hipLaunchKernelGGL(gqa_kernel, dim3(256), dim3(384), LDS_BYTES, stream, (const __hip_bfloat16*)d_in[0], (const __hip_bfloat16*)d_in[1],
                     (const __hip_bfloat16*)d_in[2], (__hip_bfloat16*)d_out, (char*)d_ws);
}
```

```cpp
#define PROBE 1
#define PROBE_BID 100
#include <hip/hip_runtime.h>
#include <hip/hip_bf16.h>

typedef short bf16x8 __attribute__((ext_vector_type(8)));
typedef short s16x4 __attribute__((ext_vector_type(4)));
typedef float f32x16 __attribute__((ext_vector_type(16)));
typedef float f32x4 __attribute__((ext_vector_type(4)));
typedef float f32x2 __attribute__((ext_vector_type(2)));
typedef __bf16 bf16x2_t __attribute__((ext_vector_type(2)));
typedef unsigned u32x4 __attribute__((ext_vector_type(4)));
typedef unsigned u32x2 __attribute__((ext_vector_type(2)));
typedef __attribute__((address_space(3))) s16x4* lds_s16x4_p;
typedef __attribute__((address_space(3))) bf16x8* lds_bf16x8_p;
typedef __attribute__((address_space(3))) f32x4* lds_f32x4_p;
typedef __attribute__((address_space(3))) f32x2* lds_f32x2_p;
typedef __attribute__((address_space(1))) unsigned long long gu64;

constexpr int SQ = 32, SK = 2048, D = 128;
constexpr int NSPLIT = 4, KT = 32, KPW = SK / NSPLIT, NT = KPW / KT;
constexpr int NS = 8;
#define LA 4
static_assert(LA <= NS - 3, "lookahead");
constexpr int TILE_B = KT * D * 2;
constexpr int LDS_BYTES = NS * 2 * TILE_B;
constexpr float SC = 0.088388347648318447f * 1.4426950408889634f;
constexpr float THR = 8.f;
constexpr size_t WS_E = 0, WS_F = 4096, WS_ML = 65536, WS_O = 1 << 20;
constexpr size_t WS_END = WS_O + (size_t)64 * 4 * 4 * 16384;

__device__ __forceinline__ unsigned pk_bf16(float a, float b) {
  f32x2 v = {a, b};
  bf16x2_t r = __builtin_convertvector(v, bf16x2_t);
  return __builtin_bit_cast(unsigned, r);
}

__device__ __forceinline__ void dma8(const char* gbase, unsigned lds, unsigned o0, unsigned o1, unsigned o2, unsigned o3) {
  unsigned keep;
  const char* g2 = gbase + 4096;
  const unsigned lds2 = lds + 4096;
  asm volatile(
      "s_mov_b32 %0, m0\n\t"
      "s_mov_b32 m0, %7\n\t"
      "s_nop 0\n\t"
      "global_load_lds_dwordx4 %1, %5 offset:0 nt\n\t"
      "global_load_lds_dwordx4 %2, %5 offset:1024 nt\n\t"
      "global_load_lds_dwordx4 %3, %5 offset:2048 nt\n\t"
      "global_load_lds_dwordx4 %4, %5 offset:3072 nt\n\t"
      "s_mov_b32 m0, %8\n\t"
      "s_nop 0\n\t"
      "global_load_lds_dwordx4 %1, %6 offset:0 nt\n\t"
      "global_load_lds_dwordx4 %2, %6 offset:1024 nt\n\t"
      "global_load_lds_dwordx4 %3, %6 offset:2048 nt\n\t"
      "global_load_lds_dwordx4 %4, %6 offset:3072 nt\n\t"
      "s_mov_b32 m0, %0"
      : "=&s"(keep)
      : "v"(o0), "v"(o1), "v"(o2), "v"(o3), "s"(gbase), "s"(g2), "s"(lds), "s"(lds2)
      : "memory");
}

template <int N> __device__ __forceinline__ void wait_bar() { asm volatile("s_waitcnt vmcnt(%0)\n\ts_barrier" ::"i"(N) : "memory"); }
#define RAMP 2
template <int T, int ISS> __device__ __forceinline__ void loader_steps(const char* src, unsigned dst0, unsigned o0, unsigned o1, unsigned o2, unsigned o3) {
  if constexpr (T < NT) {
    wait_bar<8 * (ISS - T - 1)>();
    constexpr int want = T + 1 + LA < NT ? T + 1 + LA : NT;
    constexpr int nxt = ISS + RAMP < want ? ISS + RAMP : (want > ISS ? want : ISS);
#pragma unroll
    for (int t = ISS; t < nxt; ++t) dma8(src + (size_t)t * TILE_B, dst0 + (t % NS) * 2 * TILE_B, o0, o1, o2, o3);
    loader_steps<T + 1, nxt>(src, dst0, o0, o1, o2, o3);
  }
}
template <int I> __device__ __forceinline__ void tail_waits() {
  if constexpr (I >= 0) { wait_bar<8 * I>(); tail_waits<I - 1>(); }
}

__global__ __launch_bounds__(384, 2) void gqa_kernel(const __hip_bfloat16* __restrict__ Qp, const __hip_bfloat16* __restrict__ Kp,
                                                      const __hip_bfloat16* __restrict__ Vp, __hip_bfloat16* __restrict__ Op, char* __restrict__ ws) {
  extern __shared__ __attribute__((aligned(16))) char smem[];
  const int tid = threadIdx.x, lane = tid & 63;
  const int wv = __builtin_amdgcn_readfirstlane(tid >> 6);
  const int bid = blockIdx.x, xcd = bid & 7, idx = bid >> 3;
  const int unit = xcd * 8 + (idx >> 2), g = idx & 3;
  const unsigned smem_base = (unsigned)(uintptr_t)smem;
  unsigned long long st0 = __builtin_amdgcn_s_memtime(), rt0 = __builtin_amdgcn_s_memrealtime();

  if (__builtin_expect(wv < 2, 1)) {
    const int li = lane & 15, lq = lane >> 4;
    const unsigned sw = wv == 0 ? lq : (lq << 2);
    const unsigned o0 = lq * 256 + ((li ^ sw) * 16);
    const unsigned x = wv == 0 ? 64 : 0;
    const unsigned o1 = o0 ^ x, o2 = o0 ^ (2 * x), o3 = o0 ^ (3 * x);
    const char* src = (wv == 0 ? (const char*)Kp : (const char*)Vp) + ((size_t)unit * SK + (size_t)g * KPW) * D * 2;
    const unsigned dst0 = smem_base + wv * TILE_B;
#define P0 LA
#pragma unroll
    for (int t = 0; t < P0; ++t) dma8(src + (size_t)t * TILE_B, dst0 + t * 2 * TILE_B, o0, o1, o2, o3);
    loader_steps<0, P0>(src, dst0, o0, o1, o2, o3);
    asm volatile("s_barrier" ::: "memory");
    return;
  }
  const int wid = wv - 2;
  const int r32 = lane & 31, h = lane >> 5;
  bf16x8 qf[8];
  {
    const char* Qb = (const char*)Qp + (size_t)(unit * 4 + wid) * SQ * D * 2 + r32 * 256 + h * 16;
#pragma unroll
    for (int ks = 0; ks < 8; ++ks) {
      const u32x4 raw = *(const u32x4*)(Qb + ks * 32);
      u32x4 w;
#pragma unroll
      for (int e = 0; e < 4; ++e)
        w[e] = pk_bf16(__uint_as_float(raw[e] << 16) * SC, __uint_as_float(raw[e] & 0xffff0000u) * SC);
      qf[ks] = __builtin_bit_cast(bf16x8, w);
    }
  }
  const unsigned long long tag_pub = *(const unsigned long long*)(ws + WS_E + (unit * 4 + wid) * 8) + 1;
  const unsigned long long tag_own = *(const unsigned long long*)(ws + WS_E + (unit * 4 + g) * 8) + 1;

  const unsigned kr = smem_base + r32 * 256 + ((h ^ (r32 & 15)) * 16);
  const int tq = (lane & 15) >> 2, tp = lane & 3, tblk = (lane >> 4) & 1;
  const unsigned vr = smem_base + TILE_B + 256 * (4 * h + tq) + 32 * tblk + 8 * tp;

  f32x16 o[4];
#pragma unroll
  for (int i = 0; i < 4; ++i)
#pragma unroll
    for (int j = 0; j < 16; ++j) o[i][j] = 0.f;
  float m, l = 0.f;
  f32x16 sa, sb, negm;
  bf16x8 kf[8];
  s16x4 vlo[8], vhi[8];
  unsigned ka[8], va[4];
#pragma unroll
  for (int ks = 0; ks < 8; ++ks) ka[ks] = kr ^ (32 * ks);
#pragma unroll
  for (int db = 0; db < 4; ++db) va[db] = vr + 64 * (db ^ tq);
#define SB() __builtin_amdgcn_sched_barrier(0)
#define PIN(v) asm volatile("" : "+v"(v))
#define KREAD(ks, tile) kf[ks] = *(lds_bf16x8_p)(uintptr_t)(ka[ks] + ((tile) & 3) * 2 * TILE_B)
#define VREAD(i, tile)                                                                                                                         \
  do {                                                                                                                                         \
    vlo[i] = __builtin_amdgcn_ds_read_tr16_b64_v4i16((lds_s16x4_p)(uintptr_t)(va[(i) & 3] + 4096 * ((i) >> 2) + ((tile) & 3) * 2 * TILE_B));        \
    vhi[i] = __builtin_amdgcn_ds_read_tr16_b64_v4i16((lds_s16x4_p)(uintptr_t)(va[(i) & 3] + 4096 * ((i) >> 2) + 2048 + ((tile) & 3) * 2 * TILE_B)); \
  } while (0)
  {
    const f32x16 zero = {};
    asm volatile("s_barrier" ::: "memory");
#pragma unroll
    for (int ks = 0; ks < 8; ++ks) KREAD(ks, 0);
    sa = __builtin_amdgcn_mfma_f32_32x32x16_bf16(kf[0], qf[0], zero, 0, 0, 0);
#pragma unroll
    for (int ks = 1; ks < 8; ++ks) sa = __builtin_amdgcn_mfma_f32_32x32x16_bf16(kf[ks], qf[ks], sa, 0, 0, 0);
    SB();
    asm volatile("s_barrier" ::: "memory");
#pragma unroll
    for (int ks = 0; ks < 8; ++ks) KREAD(ks, 1);
    float mx = sa[0];
#pragma unroll
    for (int j = 1; j < 16; ++j) mx = fmaxf(mx, sa[j]);
    auto rr = __builtin_amdgcn_permlane32_swap(__float_as_uint(mx), __float_as_uint(mx), false, false);
    m = fmaxf(__uint_as_float(rr[0]), __uint_as_float(rr[1]));
#pragma unroll
    for (int j = 0; j < 16; ++j) { sa[j] -= m; negm[j] = -m; }
    SB();
  }
#define ITER(t, S, X)                                                                                              \
  {                                                                                                                \
    unsigned pw[8];                                                                                                \
    _Pragma("unroll") for (int k = 0; k < 8; ++k) {                                                                \
      if (k == 0) S = __builtin_amdgcn_mfma_f32_32x32x16_bf16(kf[0], qf[0], negm, 0, 0, 0);                        \
      else S = __builtin_amdgcn_mfma_f32_32x32x16_bf16(kf[k], qf[k], S, 0, 0, 0);                                  \
      SB();                                                                                                        \
      VREAD(k, (t) - 1);                                                                                           \
      X[2 * k] = __builtin_amdgcn_exp2f(X[2 * k]);                                                                 \
      X[2 * k + 1] = __builtin_amdgcn_exp2f(X[2 * k + 1]);                                                         \
      if (k > 0) {                                                                                                 \
        pw[k - 1] = pk_bf16(X[2 * k - 2], X[2 * k - 1]);                                                           \
        if (k & 1) { l += X[2 * k - 2]; PIN(l); }                                                                  \
      }                                                                                                            \
      SB();                                                                                                        \
    }                                                                                                              \
    pw[7] = pk_bf16(X[14], X[15]);                                                                                 \
    asm volatile("s_barrier" ::: "memory");                     \
    const bf16x8 pf0 = __builtin_bit_cast(bf16x8, u32x4{pw[0], pw[1], pw[2], pw[3]});                              \
    const bf16x8 pf1 = __builtin_bit_cast(bf16x8, u32x4{pw[4], pw[5], pw[6], pw[7]});                              \
    float mx = 0.f;                                                                                                \
    SB();                                                                                                          \
    _Pragma("unroll") for (int k = 0; k < 8; ++k) {                                                                \
      const bf16x8 vf = __builtin_shufflevector(vlo[k], vhi[k], 0, 1, 2, 3, 4, 5, 6, 7);                           \
      o[k & 3] = __builtin_amdgcn_mfma_f32_32x32x16_bf16(vf, k < 4 ? pf0 : pf1, o[k & 3], 0, 0, 0);                \
      SB();                                                                                                        \
      if ((((t) + 1) & 3) == 0) { ka[k] += ((((t) + 1) & 4) ? 8 * TILE_B : -8 * TILE_B); PIN(ka[k]); }                                               \
      if ((t) + 1 < NT) KREAD(k, (t) + 1);                                                                         \
      if ((((t)) & 3) == 0 && k < 4) { va[k] += (((t) & 4) ? 8 * TILE_B : -8 * TILE_B); PIN(va[k]); }                                          \
                                             \
      l += X[2 * k + 1];                                                                                           \
      if (k & 1) l += X[2 * k];                                                                                    \
      PIN(l);                                                                                                      \
      if (k == 2) { PIN(S); mx = fmaxf(fmaxf(S[0], S[1]), S[2]); mx = fmaxf(fmaxf(mx, S[3]), S[4]); PIN(mx); }     \
      if (k == 3) { mx = fmaxf(fmaxf(mx, S[5]), S[6]); mx = fmaxf(fmaxf(mx, S[7]), S[8]); PIN(mx); }               \
      if (k == 4) { mx = fmaxf(fmaxf(mx, S[9]), S[10]); mx = fmaxf(fmaxf(mx, S[11]), S[12]); PIN(mx); }            \
      if (k == 5) { mx = fmaxf(fmaxf(mx, S[13]), S[14]); mx = fmaxf(mx, S[15]); PIN(mx); }                         \
      if (k == 6) {                                                                                                \
        auto rr = __builtin_amdgcn_permlane32_swap(__float_as_uint(mx), __float_as_uint(mx), false, false);        \
        mx = fmaxf(__uint_as_float(rr[0]), __uint_as_float(rr[1]));                                                \
      }                                                                                                            \
      SB();                                                                                                        \
    }                                                                                                              \
    if (RESCALE_COND(mx)) {   \
      const float d = fmaxf(mx, 0.f);                                                                              \
      const float alpha = __builtin_amdgcn_exp2f(-d);                                                              \
      m += d;                                                                                                      \
      l *= alpha;                                                                                                  \
      _Pragma("unroll") for (int j = 0; j < 16; ++j) { S[j] -= d; negm[j] = -m; }                                  \
      _Pragma("unroll") for (int i = 0; i < 4; ++i) _Pragma("unroll") for (int j = 0; j < 16; ++j) o[i][j] *= alpha; \
    }                                                                                                              \
    SB();                                                                                                          \
  }
#define RESCALE_COND(mx) (__any(!(mx <= THR)))
  SB(); unsigned long long ra = __builtin_amdgcn_s_memrealtime(); __builtin_amdgcn_s_waitcnt(0xC07F); SB();
  ITER(1, sb, sa) ITER(2, sa, sb) ITER(3, sb, sa) ITER(4, sa, sb) ITER(5, sb, sa) ITER(6, sa, sb) ITER(7, sb, sa) ITER(8, sa, sb)
  ITER(9, sb, sa) ITER(10, sa, sb) ITER(11, sb, sa) ITER(12, sa, sb) ITER(13, sb, sa) ITER(14, sa, sb) ITER(15, sb, sa)
  {
#pragma unroll
    for (int k = 0; k < 8; ++k) VREAD(k, NT - 1);
#pragma unroll
    for (int j = 0; j < 16; ++j) sb[j] = __builtin_amdgcn_exp2f(sb[j]);
    const bf16x8 pf0 = __builtin_bit_cast(bf16x8, u32x4{pk_bf16(sb[0], sb[1]), pk_bf16(sb[2], sb[3]), pk_bf16(sb[4], sb[5]), pk_bf16(sb[6], sb[7])});
    const bf16x8 pf1 = __builtin_bit_cast(bf16x8, u32x4{pk_bf16(sb[8], sb[9]), pk_bf16(sb[10], sb[11]), pk_bf16(sb[12], sb[13]), pk_bf16(sb[14], sb[15])});
#pragma unroll
    for (int k = 0; k < 8; ++k) {
      const bf16x8 vf = __builtin_shufflevector(vlo[k], vhi[k], 0, 1, 2, 3, 4, 5, 6, 7);
      o[k & 3] = __builtin_amdgcn_mfma_f32_32x32x16_bf16(vf, k < 4 ? pf0 : pf1, o[k & 3], 0, 0, 0);
    }
#pragma unroll
    for (int j = 0; j < 16; ++j) l += sb[j];
  }

  asm volatile("" :: "v"(o[0][0]), "v"(o[1][0]), "v"(o[2][0]), "v"(o[3][0]));
  unsigned long long rb = __builtin_amdgcn_s_memrealtime();
  {
    auto rr = __builtin_amdgcn_permlane32_swap(__float_as_uint(l), __float_as_uint(l), false, false);
    l = __uint_as_float(rr[0]) + __uint_as_float(rr[1]);
  }
  const auto rsrc = __builtin_amdgcn_make_buffer_rsrc((void*)ws, 0, (int)WS_END, 0x00020000);
  const unsigned xo = smem_base;
  const unsigned xml = smem_base + 16384;
  if (wid != g) {
    const int slab = (unit * 4 + wid) * 4 + g;
    const unsigned ob = (unsigned)(WS_O + (size_t)slab * 16384) + lane * 16;
#pragma unroll
    for (int db = 0; db < 4; ++db)
#pragma unroll
      for (int gq = 0; gq < 4; ++gq) {
        u32x4 v = {__float_as_uint(o[db][4 * gq]), __float_as_uint(o[db][4 * gq + 1]), __float_as_uint(o[db][4 * gq + 2]), __float_as_uint(o[db][4 * gq + 3])};
        __builtin_amdgcn_raw_buffer_store_b128(v, rsrc, ob + (db * 4 + gq) * 1024, 0, 16);
      }
    if (h == 0) {
      u32x2 v = {__float_as_uint(m), __float_as_uint(l)};
      __builtin_amdgcn_raw_buffer_store_b64(v, rsrc, (unsigned)(WS_ML + slab * 256) + r32 * 8, 0, 16);
    }
    asm volatile("s_waitcnt vmcnt(0)" ::: "memory");
    if (lane == 0) __hip_atomic_store((gu64*)(ws + WS_F + slab * 8), tag_pub, __ATOMIC_RELAXED, __HIP_MEMORY_SCOPE_AGENT);
  } else {
#pragma unroll
    for (int db = 0; db < 4; ++db)
#pragma unroll
      for (int gq = 0; gq < 4; ++gq) {
        f32x4 v = {o[db][4 * gq], o[db][4 * gq + 1], o[db][4 * gq + 2], o[db][4 * gq + 3]};
        *(lds_f32x4_p)(uintptr_t)(xo + (db * 4 + gq) * 1024 + lane * 16) = v;
      }
    if (h == 0) {
      f32x2 v = {m, l};
      *(lds_f32x2_p)(uintptr_t)(xml + r32 * 8) = v;
    }
  }
  __syncthreads();
  {
    const gu64* fp = (const gu64*)(ws + WS_F + (size_t)((unit * 4 + g) * 4) * 8);
    const int sl = lane & 3;
#pragma unroll 1
    for (unsigned spins = 0; spins < 20000000u; ++spins) {
      const unsigned long long v = __hip_atomic_load(fp + sl, __ATOMIC_RELAXED, __HIP_MEMORY_SCOPE_AGENT);
      const bool ok = (sl == g) || (v == tag_own);
      if (__all(ok)) break;
      __builtin_amdgcn_s_sleep(1);
    }
    __builtin_amdgcn_fence(__ATOMIC_ACQUIRE, "wavefront");
  }
  {
    f32x2 ml0 = *(lds_f32x2_p)(uintptr_t)(xml + r32 * 8);
    u32x4 ro[3][4];
    u32x2 rml[3];
#pragma unroll
    for (int i = 0; i < 3; ++i) {
      const int sg = (g + 1 + i) & 3;
      const int slab = (unit * 4 + g) * 4 + sg;
      rml[i] = __builtin_amdgcn_raw_buffer_load_b64(rsrc, (unsigned)(WS_ML + slab * 256) + r32 * 8, 0, 16);
#pragma unroll
      for (int gq = 0; gq < 4; ++gq)
        ro[i][gq] = __builtin_amdgcn_raw_buffer_load_b128(rsrc, (unsigned)(WS_O + (size_t)slab * 16384) + lane * 16 + (wid * 4 + gq) * 1024, 0, 16);
    }
    f32x4 own[4];
#pragma unroll
    for (int gq = 0; gq < 4; ++gq) own[gq] = *(lds_f32x4_p)(uintptr_t)(xo + (wid * 4 + gq) * 1024 + lane * 16);
    float M = ml0[0];
#pragma unroll
    for (int i = 0; i < 3; ++i) M = fmaxf(M, __uint_as_float(rml[i][0]));
    const float f0 = __builtin_amdgcn_exp2f(ml0[0] - M);
    float L = f0 * ml0[1];
    float f[3];
#pragma unroll
    for (int i = 0; i < 3; ++i) {
      f[i] = __builtin_amdgcn_exp2f(__uint_as_float(rml[i][0]) - M);
      L += f[i] * __uint_as_float(rml[i][1]);
    }
    const float inv = 1.f / L;
    char* orow = (char*)Op + ((size_t)(unit * 4 + g) * SQ + r32) * D * 2 + wid * 64 + h * 8;
#pragma unroll
    for (int gq = 0; gq < 4; ++gq) {
      float a[4];
#pragma unroll
      for (int e = 0; e < 4; ++e) {
        float x = f0 * own[gq][e];
#pragma unroll
        for (int i = 0; i < 3; ++i) x += f[i] * __uint_as_float(ro[i][gq][e]);
        a[e] = x * inv;
      }
      u32x2 w = {pk_bf16(a[0], a[1]), pk_bf16(a[2], a[3])};
      *(u32x2*)(orow + gq * 16) = w;
    }
  }
  {
    unsigned long long rc = __builtin_amdgcn_s_memrealtime(), sc = __builtin_amdgcn_s_memtime();
    if (bid == PROBE_BID && wid == 0) {
      unsigned long long d = PROBE == 1 ? 10 * (ra - rt0) : PROBE == 2 ? 10 * (rb - ra) : PROBE == 3 ? 10 * (rc - rb) : PROBE == 4 ? (sc - st0) / 10 : PROBE == 5 ? 10 * (rc - rt0) : 0;
      while (__builtin_amdgcn_s_memrealtime() < rc + d) __builtin_amdgcn_s_sleep(2);
    }
  }
  __syncthreads();
  if (tid == 128) *(unsigned long long*)(ws + WS_E + (unit * 4 + g) * 8) = tag_own;
}

extern "C" void kernel_launch(void* const* d_in, const int* in_sizes, int n_in, void* d_out, int out_size, void* d_ws, size_t ws_size,
                              hipStream_t stream) {
  (void)in_sizes; (void)n_in; (void)out_size; (void)ws_size;
  (void)hipFuncSetAttribute((const void*)gqa_kernel, hipFuncAttributeMaxDynamicSharedMemorySize, LDS_BYTES);
  hipLaunchKernelGGL(gqa_kernel, dim3(256), dim3(384), LDS_BYTES, stream, (const __hip_bfloat16*)d_in[0], (const __hip_bfloat16*)d_in[1],
                     (const __hip_bfloat16*)d_in[2], (__hip_bfloat16*)d_out, (char*)d_ws);
}
```

```cpp
#include <hip/hip_runtime.h>
#include <hip/hip_bf16.h>

typedef short bf16x8 __attribute__((ext_vector_type(8)));
typedef short s16x4 __attribute__((ext_vector_type(4)));
typedef float f32x16 __attribute__((ext_vector_type(16)));
typedef float f32x4 __attribute__((ext_vector_type(4)));
typedef float f32x2 __attribute__((ext_vector_type(2)));
typedef __bf16 bf16x2_t __attribute__((ext_vector_type(2)));
typedef unsigned u32x4 __attribute__((ext_vector_type(4)));
typedef unsigned u32x2 __attribute__((ext_vector_type(2)));
typedef __attribute__((address_space(3))) s16x4* lds_s16x4_p;
typedef __attribute__((address_space(3))) bf16x8* lds_bf16x8_p;
typedef __attribute__((address_space(3))) f32x4* lds_f32x4_p;
typedef __attribute__((address_space(3))) f32x2* lds_f32x2_p;
typedef __attribute__((address_space(1))) unsigned long long gu64;

constexpr int SQ = 32, SK = 2048, D = 128;
constexpr int NSPLIT = 4, KT = 32, KPW = SK / NSPLIT, NT = KPW / KT;
constexpr int NS = 8;
#define LA 4
static_assert(LA <= NS - 3, "lookahead");
constexpr int TILE_B = KT * D * 2;
constexpr int LDS_BYTES = NS * 2 * TILE_B;
constexpr float SC = 0.088388347648318447f * 1.4426950408889634f;
constexpr float THR = 8.f;
constexpr size_t WS_E = 0, WS_F = 4096, WS_ML = 65536, WS_O = 1 << 20;
constexpr size_t WS_END = WS_O + (size_t)64 * 4 * 4 * 16384;

__device__ __forceinline__ unsigned pk_bf16(float a, float b) {
  f32x2 v = {a, b};
  bf16x2_t r = __builtin_convertvector(v, bf16x2_t);
  return __builtin_bit_cast(unsigned, r);
}

__device__ __forceinline__ void dma8(const char* gbase, unsigned lds, unsigned o0, unsigned o1, unsigned o2, unsigned o3) {
  unsigned keep;
  const char* g2 = gbase + 4096;
  const unsigned lds2 = lds + 4096;
  asm volatile(
      "s_mov_b32 %0, m0\n\t"
      "s_mov_b32 m0, %7\n\t"
      "s_nop 0\n\t"
      "global_load_lds_dwordx4 %1, %5 offset:0 nt\n\t"
      "global_load_lds_dwordx4 %2, %5 offset:1024 nt\n\t"
      "global_load_lds_dwordx4 %3, %5 offset:2048 nt\n\t"
      "global_load_lds_dwordx4 %4, %5 offset:3072 nt\n\t"
      "s_mov_b32 m0, %8\n\t"
      "s_nop 0\n\t"
      "global_load_lds_dwordx4 %1, %6 offset:0 nt\n\t"
      "global_load_lds_dwordx4 %2, %6 offset:1024 nt\n\t"
      "global_load_lds_dwordx4 %3, %6 offset:2048 nt\n\t"
      "global_load_lds_dwordx4 %4, %6 offset:3072 nt\n\t"
      "s_mov_b32 m0, %0"
      : "=&s"(keep)
      : "v"(o0), "v"(o1), "v"(o2), "v"(o3), "s"(gbase), "s"(g2), "s"(lds), "s"(lds2)
      : "memory");
}

template <int N> __device__ __forceinline__ void wait_bar() { asm volatile("s_waitcnt vmcnt(%0)\n\ts_barrier" ::"i"(N) : "memory"); }
#define RAMP 2
template <int T, int ISS> __device__ __forceinline__ void loader_steps(const char* src, unsigned dst0, unsigned o0, unsigned o1, unsigned o2, unsigned o3) {
  if constexpr (T < NT) {
    wait_bar<8 * (ISS - T - 1)>();
    constexpr int want = T + 1 + LA < NT ? T + 1 + LA : NT;
    constexpr int nxt = ISS + RAMP < want ? ISS + RAMP : (want > ISS ? want : ISS);
#pragma unroll
    for (int t = ISS; t < nxt; ++t) dma8(src + (size_t)t * TILE_B, dst0 + (t % NS) * 2 * TILE_B, o0, o1, o2, o3);
    loader_steps<T + 1, nxt>(src, dst0, o0, o1, o2, o3);
  }
}
template <int I> __device__ __forceinline__ void tail_waits() {
  if constexpr (I >= 0) { wait_bar<8 * I>(); tail_waits<I - 1>(); }
}

__global__ __launch_bounds__(384, 2) void gqa_kernel(const __hip_bfloat16* __restrict__ Qp, const __hip_bfloat16* __restrict__ Kp,
                                                      const __hip_bfloat16* __restrict__ Vp, __hip_bfloat16* __restrict__ Op, char* __restrict__ ws) {
  extern __shared__ __attribute__((aligned(16))) char smem[];
  const int tid = threadIdx.x, lane = tid & 63;
  const int wv = __builtin_amdgcn_readfirstlane(tid >> 6);
  const int bid = blockIdx.x, xcd = bid & 7, idx = bid >> 3;
  const int unit = xcd * 8 + (idx >> 2), g = idx & 3;
  const unsigned smem_base = (unsigned)(uintptr_t)smem;

  if (__builtin_expect(wv < 2, 1)) {
    const int li = lane & 15, lq = lane >> 4;
    const unsigned sw = wv == 0 ? lq : (lq << 2);
    const unsigned o0 = lq * 256 + ((li ^ sw) * 16);
    const unsigned x = wv == 0 ? 64 : 0;
    const unsigned o1 = o0 ^ x, o2 = o0 ^ (2 * x), o3 = o0 ^ (3 * x);
    const char* src = (wv == 0 ? (const char*)Kp : (const char*)Vp) + ((size_t)unit * SK + (size_t)g * KPW) * D * 2;
    const unsigned dst0 = smem_base + wv * TILE_B;
#define P0 LA
#pragma unroll
    for (int t = 0; t < P0; ++t) dma8(src + (size_t)t * TILE_B, dst0 + t * 2 * TILE_B, o0, o1, o2, o3);
    loader_steps<0, P0>(src, dst0, o0, o1, o2, o3);
    asm volatile("s_barrier" ::: "memory");
    return;
  }
  const int wid = wv - 2;
  const int r32 = lane & 31, h = lane >> 5;
  bf16x8 qf[8];
  {
    const char* Qb = (const char*)Qp + (size_t)(unit * 4 + wid) * SQ * D * 2 + r32 * 256 + h * 16;
#pragma unroll
    for (int ks = 0; ks < 8; ++ks) {
      const u32x4 raw = *(const u32x4*)(Qb + ks * 32);
      u32x4 w;
#pragma unroll
      for (int e = 0; e < 4; ++e)
        w[e] = pk_bf16(__uint_as_float(raw[e] << 16) * SC, __uint_as_float(raw[e] & 0xffff0000u) * SC);
      qf[ks] = __builtin_bit_cast(bf16x8, w);
    }
  }
  const unsigned long long tag_pub = *(const unsigned long long*)(ws + WS_E + (unit * 4 + wid) * 8) + 1;
  const unsigned long long tag_own = *(const unsigned long long*)(ws + WS_E + (unit * 4 + g) * 8) + 1;

  const unsigned kr = smem_base + r32 * 256 + ((h ^ (r32 & 15)) * 16);
  const int tq = (lane & 15) >> 2, tp = lane & 3, tblk = (lane >> 4) & 1;
  const unsigned vr = smem_base + TILE_B + 256 * (4 * h + tq) + 32 * tblk + 8 * tp;

  f32x16 o[4];
#pragma unroll
  for (int i = 0; i < 4; ++i)
#pragma unroll
    for (int j = 0; j < 16; ++j) o[i][j] = 0.f;
  float m, l = 0.f;
  f32x16 sa, sb, negm;
  bf16x8 kf[8];
  s16x4 vlo[8], vhi[8];
  unsigned ka[8], va[4];
#pragma unroll
  for (int ks = 0; ks < 8; ++ks) ka[ks] = kr ^ (32 * ks);
#pragma unroll
  for (int db = 0; db < 4; ++db) va[db] = vr + 64 * (db ^ tq);
#define SB() __builtin_amdgcn_sched_barrier(0)
#define PIN(v) asm volatile("" : "+v"(v))
#define KREAD(ks, tile) kf[ks] = *(lds_bf16x8_p)(uintptr_t)(ka[ks] + ((tile) & 3) * 2 * TILE_B)
#define VREAD(i, tile)                                                                                                                         \
  do {                                                                                                                                         \
    vlo[i] = __builtin_amdgcn_ds_read_tr16_b64_v4i16((lds_s16x4_p)(uintptr_t)(va[(i) & 3] + 4096 * ((i) >> 2) + ((tile) & 3) * 2 * TILE_B));        \
    vhi[i] = __builtin_amdgcn_ds_read_tr16_b64_v4i16((lds_s16x4_p)(uintptr_t)(va[(i) & 3] + 4096 * ((i) >> 2) + 2048 + ((tile) & 3) * 2 * TILE_B)); \
  } while (0)
  {
    const f32x16 zero = {};
    asm volatile("s_barrier" ::: "memory");
#pragma unroll
    for (int ks = 0; ks < 8; ++ks) KREAD(ks, 0);
    sa = __builtin_amdgcn_mfma_f32_32x32x16_bf16(kf[0], qf[0], zero, 0, 0, 0);
#pragma unroll
    for (int ks = 1; ks < 8; ++ks) sa = __builtin_amdgcn_mfma_f32_32x32x16_bf16(kf[ks], qf[ks], sa, 0, 0, 0);
    SB();
    asm volatile("s_barrier" ::: "memory");
#pragma unroll
    for (int ks = 0; ks < 8; ++ks) KREAD(ks, 1);
    float mx = sa[0];
#pragma unroll
    for (int j = 1; j < 16; ++j) mx = fmaxf(mx, sa[j]);
    auto rr = __builtin_amdgcn_permlane32_swap(__float_as_uint(mx), __float_as_uint(mx), false, false);
    m = fmaxf(__uint_as_float(rr[0]), __uint_as_float(rr[1]));
#pragma unroll
    for (int j = 0; j < 16; ++j) { sa[j] -= m; negm[j] = -m; }
    SB();
  }
#define ITER(t, S, X)                                                                                              \
  {                                                                                                                \
    unsigned pw[8];                                                                                                \
    _Pragma("unroll") for (int k = 0; k < 8; ++k) {                                                                \
      if (k == 0) S = __builtin_amdgcn_mfma_f32_32x32x16_bf16(kf[0], qf[0], negm, 0, 0, 0);                        \
      else S = __builtin_amdgcn_mfma_f32_32x32x16_bf16(kf[k], qf[k], S, 0, 0, 0);                                  \
      SB();                                                                                                        \
      VREAD(k, (t) - 1);                                                                                           \
      X[2 * k] = __builtin_amdgcn_exp2f(X[2 * k]);                                                                 \
      X[2 * k + 1] = __builtin_amdgcn_exp2f(X[2 * k + 1]);                                                         \
      if (k > 0) {                                                                                                 \
        pw[k - 1] = pk_bf16(X[2 * k - 2], X[2 * k - 1]);                                                           \
        if (k & 1) { l += X[2 * k - 2]; PIN(l); }                                                                  \
      }                                                                                                            \
      SB();                                                                                                        \
    }                                                                                                              \
    pw[7] = pk_bf16(X[14], X[15]);                                                                                 \
    asm volatile("s_barrier" ::: "memory");                     \
    const bf16x8 pf0 = __builtin_bit_cast(bf16x8, u32x4{pw[0], pw[1], pw[2], pw[3]});                              \
    const bf16x8 pf1 = __builtin_bit_cast(bf16x8, u32x4{pw[4], pw[5], pw[6], pw[7]});                              \
    float mx = 0.f;                                                                                                \
    SB();                                                                                                          \
    _Pragma("unroll") for (int k = 0; k < 8; ++k) {                                                                \
      const bf16x8 vf = __builtin_shufflevector(vlo[k], vhi[k], 0, 1, 2, 3, 4, 5, 6, 7);                           \
      o[k & 3] = __builtin_amdgcn_mfma_f32_32x32x16_bf16(vf, k < 4 ? pf0 : pf1, o[k & 3], 0, 0, 0);                \
      SB();                                                                                                        \
      if ((((t) + 1) & 3) == 0) { ka[k] += ((((t) + 1) & 4) ? 8 * TILE_B : -8 * TILE_B); PIN(ka[k]); }                                               \
      if ((t) + 1 < NT) KREAD(k, (t) + 1);                                                                         \
      if ((((t)) & 3) == 0 && k < 4) { va[k] += (((t) & 4) ? 8 * TILE_B : -8 * TILE_B); PIN(va[k]); }                                          \
                                             \
      l += X[2 * k + 1];                                                                                           \
      if (k & 1) l += X[2 * k];                                                                                    \
      PIN(l);                                                                                                      \
      if (k == 2) { PIN(S); mx = fmaxf(fmaxf(S[0], S[1]), S[2]); mx = fmaxf(fmaxf(mx, S[3]), S[4]); PIN(mx); }     \
      if (k == 3) { mx = fmaxf(fmaxf(mx, S[5]), S[6]); mx = fmaxf(fmaxf(mx, S[7]), S[8]); PIN(mx); }               \
      if (k == 4) { mx = fmaxf(fmaxf(mx, S[9]), S[10]); mx = fmaxf(fmaxf(mx, S[11]), S[12]); PIN(mx); }            \
      if (k == 5) { mx = fmaxf(fmaxf(mx, S[13]), S[14]); mx = fmaxf(mx, S[15]); PIN(mx); }                         \
      if (k == 6) {                                                                                                \
        auto rr = __builtin_amdgcn_permlane32_swap(__float_as_uint(mx), __float_as_uint(mx), false, false);        \
        mx = fmaxf(__uint_as_float(rr[0]), __uint_as_float(rr[1]));                                                \
      }                                                                                                            \
      SB();                                                                                                        \
    }                                                                                                              \
    if (RESCALE_COND(mx)) {   \
      const float d = fmaxf(mx, 0.f);                                                                              \
      const float alpha = __builtin_amdgcn_exp2f(-d);                                                              \
      m += d;                                                                                                      \
      l *= alpha;                                                                                                  \
      _Pragma("unroll") for (int j = 0; j < 16; ++j) { S[j] -= d; negm[j] = -m; }                                  \
      _Pragma("unroll") for (int i = 0; i < 4; ++i) _Pragma("unroll") for (int j = 0; j < 16; ++j) o[i][j] *= alpha; \
    }                                                                                                              \
    SB();                                                                                                          \
  }
#define RESCALE_COND(mx) (__any(!(mx <= THR)))
  ITER(1, sb, sa) ITER(2, sa, sb) ITER(3, sb, sa) ITER(4, sa, sb) ITER(5, sb, sa) ITER(6, sa, sb) ITER(7, sb, sa) ITER(8, sa, sb)
  ITER(9, sb, sa) ITER(10, sa, sb) ITER(11, sb, sa) ITER(12, sa, sb) ITER(13, sb, sa) ITER(14, sa, sb) ITER(15, sb, sa)
  {
#pragma unroll
    for (int k = 0; k < 8; ++k) VREAD(k, NT - 1);
#pragma unroll
    for (int j = 0; j < 16; ++j) sb[j] = __builtin_amdgcn_exp2f(sb[j]);
    const bf16x8 pf0 = __builtin_bit_cast(bf16x8, u32x4{pk_bf16(sb[0], sb[1]), pk_bf16(sb[2], sb[3]), pk_bf16(sb[4], sb[5]), pk_bf16(sb[6], sb[7])});
    const bf16x8 pf1 = __builtin_bit_cast(bf16x8, u32x4{pk_bf16(sb[8], sb[9]), pk_bf16(sb[10], sb[11]), pk_bf16(sb[12], sb[13]), pk_bf16(sb[14], sb[15])});
#pragma unroll
    for (int k = 0; k < 8; ++k) {
      const bf16x8 vf = __builtin_shufflevector(vlo[k], vhi[k], 0, 1, 2, 3, 4, 5, 6, 7);
      o[k & 3] = __builtin_amdgcn_mfma_f32_32x32x16_bf16(vf, k < 4 ? pf0 : pf1, o[k & 3], 0, 0, 0);
    }
#pragma unroll
    for (int j = 0; j < 16; ++j) l += sb[j];
  }

  {
    auto rr = __builtin_amdgcn_permlane32_swap(__float_as_uint(l), __float_as_uint(l), false, false);
    l = __uint_as_float(rr[0]) + __uint_as_float(rr[1]);
  }
  const auto rsrc = __builtin_amdgcn_make_buffer_rsrc((void*)ws, 0, (int)WS_END, 0x00020000);
  const unsigned xo = smem_base;
  const unsigned xml = smem_base + 16384;
  if (wid != g) {
    const int slab = (unit * 4 + wid) * 4 + g;
    const unsigned ob = (unsigned)(WS_O + (size_t)slab * 8192) + lane * 16;
#pragma unroll
    for (int db = 0; db < 4; ++db)
#pragma unroll
      for (int hh = 0; hh < 2; ++hh) {
        u32x4 v = {pk_bf16(o[db][8 * hh], o[db][8 * hh + 1]), pk_bf16(o[db][8 * hh + 2], o[db][8 * hh + 3]), pk_bf16(o[db][8 * hh + 4], o[db][8 * hh + 5]),
                   pk_bf16(o[db][8 * hh + 6], o[db][8 * hh + 7])};
        __builtin_amdgcn_raw_buffer_store_b128(v, rsrc, ob + (db * 2 + hh) * 1024, 0, 16);
      }
    if (h == 0) {
      u32x2 v = {__float_as_uint(m), __float_as_uint(l)};
      __builtin_amdgcn_raw_buffer_store_b64(v, rsrc, (unsigned)(WS_ML + slab * 256) + r32 * 8, 0, 16);
    }
    asm volatile("s_waitcnt vmcnt(0)" ::: "memory");
    if (lane == 0) __hip_atomic_store((gu64*)(ws + WS_F + slab * 8), tag_pub, __ATOMIC_RELAXED, __HIP_MEMORY_SCOPE_AGENT);
  } else {
#pragma unroll
    for (int db = 0; db < 4; ++db)
#pragma unroll
      for (int gq = 0; gq < 4; ++gq) {
        f32x4 v = {o[db][4 * gq], o[db][4 * gq + 1], o[db][4 * gq + 2], o[db][4 * gq + 3]};
        *(lds_f32x4_p)(uintptr_t)(xo + (db * 4 + gq) * 1024 + lane * 16) = v;
      }
    if (h == 0) {
      f32x2 v = {m, l};
      *(lds_f32x2_p)(uintptr_t)(xml + r32 * 8) = v;
    }
  }
  __syncthreads();
  {
    const gu64* fp = (const gu64*)(ws + WS_F + (size_t)((unit * 4 + g) * 4) * 8);
    const int sl = lane & 3;
#pragma unroll 1
    for (unsigned spins = 0; spins < 20000000u; ++spins) {
      const unsigned long long v = __hip_atomic_load(fp + sl, __ATOMIC_RELAXED, __HIP_MEMORY_SCOPE_AGENT);
      const bool ok = (sl == g) || (v == tag_own);
      if (__all(ok)) break;
      __builtin_amdgcn_s_sleep(1);
    }
    __builtin_amdgcn_fence(__ATOMIC_ACQUIRE, "wavefront");
  }
  {
    f32x2 ml0 = *(lds_f32x2_p)(uintptr_t)(xml + r32 * 8);
    u32x4 ro[3][2];
    u32x2 rml[3];
#pragma unroll
    for (int i = 0; i < 3; ++i) {
      const int sg = (g + 1 + i) & 3;
      const int slab = (unit * 4 + g) * 4 + sg;
      rml[i] = __builtin_amdgcn_raw_buffer_load_b64(rsrc, (unsigned)(WS_ML + slab * 256) + r32 * 8, 0, 16);
#pragma unroll
      for (int hh = 0; hh < 2; ++hh)
        ro[i][hh] = __builtin_amdgcn_raw_buffer_load_b128(rsrc, (unsigned)(WS_O + (size_t)slab * 8192) + lane * 16 + (wid * 2 + hh) * 1024, 0, 16);
    }
    f32x4 own[4];
#pragma unroll
    for (int gq = 0; gq < 4; ++gq) own[gq] = *(lds_f32x4_p)(uintptr_t)(xo + (wid * 4 + gq) * 1024 + lane * 16);
    float M = ml0[0];
#pragma unroll
    for (int i = 0; i < 3; ++i) M = fmaxf(M, __uint_as_float(rml[i][0]));
    const float f0 = __builtin_amdgcn_exp2f(ml0[0] - M);
    float L = f0 * ml0[1];
    float f[3];
#pragma unroll
    for (int i = 0; i < 3; ++i) {
      f[i] = __builtin_amdgcn_exp2f(__uint_as_float(rml[i][0]) - M);
      L += f[i] * __uint_as_float(rml[i][1]);
    }
    const float inv = 1.f / L;
    char* orow = (char*)Op + ((size_t)(unit * 4 + g) * SQ + r32) * D * 2 + wid * 64 + h * 8;
#pragma unroll
    for (int gq = 0; gq < 4; ++gq) {
      float a[4];
#pragma unroll
      for (int e = 0; e < 4; ++e) {
        float x = f0 * own[gq][e];
#pragma unroll
        for (int i = 0; i < 3; ++i) {
          const unsigned wd = ro[i][gq >> 1][(gq & 1) * 2 + (e >> 1)];
          x += f[i] * __uint_as_float((e & 1) ? (wd & 0xffff0000u) : (wd << 16));
        }
        a[e] = x * inv;
      }
      u32x2 w = {pk_bf16(a[0], a[1]), pk_bf16(a[2], a[3])};
      *(u32x2*)(orow + gq * 16) = w;
    }
  }
  __syncthreads();
  if (tid == 128) *(unsigned long long*)(ws + WS_E + (unit * 4 + g) * 8) = tag_own;
}

extern "C" void kernel_launch(void* const* d_in, const int* in_sizes, int n_in, void* d_out, int out_size, void* d_ws, size_t ws_size,
                              hipStream_t stream) {
  (void)in_sizes; (void)n_in; (void)out_size; (void)ws_size;
  (void)hipFuncSetAttribute((const void*)gqa_kernel, hipFuncAttributeMaxDynamicSharedMemorySize, LDS_BYTES);
  hipLaunchKernelGGL(gqa_kernel, dim3(256), dim3(384), LDS_BYTES, stream, (const __hip_bfloat16*)d_in[0], (const __hip_bfloat16*)d_in[1],
                     (const __hip_bfloat16*)d_in[2], (__hip_bfloat16*)d_out, (char*)d_ws);
}
```

```cpp
#define PROBE 3
#define PROBE_BID 100
#include <hip/hip_runtime.h>
#include <hip/hip_bf16.h>

typedef short bf16x8 __attribute__((ext_vector_type(8)));
typedef short s16x4 __attribute__((ext_vector_type(4)));
typedef float f32x16 __attribute__((ext_vector_type(16)));
typedef float f32x4 __attribute__((ext_vector_type(4)));
typedef float f32x2 __attribute__((ext_vector_type(2)));
typedef __bf16 bf16x2_t __attribute__((ext_vector_type(2)));
typedef unsigned u32x4 __attribute__((ext_vector_type(4)));
typedef unsigned u32x2 __attribute__((ext_vector_type(2)));
typedef __attribute__((address_space(3))) s16x4* lds_s16x4_p;
typedef __attribute__((address_space(3))) bf16x8* lds_bf16x8_p;
typedef __attribute__((address_space(3))) f32x4* lds_f32x4_p;
typedef __attribute__((address_space(3))) f32x2* lds_f32x2_p;
typedef __attribute__((address_space(1))) unsigned long long gu64;

constexpr int SQ = 32, SK = 2048, D = 128;
constexpr int NSPLIT = 4, KT = 32, KPW = SK / NSPLIT, NT = KPW / KT;
constexpr int NS = 8;
#define LA 4
static_assert(LA <= NS - 3, "lookahead");
constexpr int TILE_B = KT * D * 2;
constexpr int LDS_BYTES = NS * 2 * TILE_B;
constexpr float SC = 0.088388347648318447f * 1.4426950408889634f;
constexpr float THR = 8.f;
constexpr size_t WS_E = 0, WS_F = 4096, WS_ML = 65536, WS_O = 1 << 20;
constexpr size_t WS_END = WS_O + (size_t)64 * 4 * 4 * 16384;

__device__ __forceinline__ unsigned pk_bf16(float a, float b) {
  f32x2 v = {a, b};
  bf16x2_t r = __builtin_convertvector(v, bf16x2_t);
  return __builtin_bit_cast(unsigned, r);
}

__device__ __forceinline__ void dma8(const char* gbase, unsigned lds, unsigned o0, unsigned o1, unsigned o2, unsigned o3) {
  unsigned keep;
  const char* g2 = gbase + 4096;
  const unsigned lds2 = lds + 4096;
  asm volatile(
      "s_mov_b32 %0, m0\n\t"
      "s_mov_b32 m0, %7\n\t"
      "s_nop 0\n\t"
      "global_load_lds_dwordx4 %1, %5 offset:0 nt\n\t"
      "global_load_lds_dwordx4 %2, %5 offset:1024 nt\n\t"
      "global_load_lds_dwordx4 %3, %5 offset:2048 nt\n\t"
      "global_load_lds_dwordx4 %4, %5 offset:3072 nt\n\t"
      "s_mov_b32 m0, %8\n\t"
      "s_nop 0\n\t"
      "global_load_lds_dwordx4 %1, %6 offset:0 nt\n\t"
      "global_load_lds_dwordx4 %2, %6 offset:1024 nt\n\t"
      "global_load_lds_dwordx4 %3, %6 offset:2048 nt\n\t"
      "global_load_lds_dwordx4 %4, %6 offset:3072 nt\n\t"
      "s_mov_b32 m0, %0"
      : "=&s"(keep)
      : "v"(o0), "v"(o1), "v"(o2), "v"(o3), "s"(gbase), "s"(g2), "s"(lds), "s"(lds2)
      : "memory");
}

template <int N> __device__ __forceinline__ void wait_bar() { asm volatile("s_waitcnt vmcnt(%0)\n\ts_barrier" ::"i"(N) : "memory"); }
#define RAMP 2
template <int T, int ISS> __device__ __forceinline__ void loader_steps(const char* src, unsigned dst0, unsigned o0, unsigned o1, unsigned o2, unsigned o3) {
  if constexpr (T < NT) {
    wait_bar<8 * (ISS - T - 1)>();
    constexpr int want = T + 1 + LA < NT ? T + 1 + LA : NT;
    constexpr int nxt = ISS + RAMP < want ? ISS + RAMP : (want > ISS ? want : ISS);
#pragma unroll
    for (int t = ISS; t < nxt; ++t) dma8(src + (size_t)t * TILE_B, dst0 + (t % NS) * 2 * TILE_B, o0, o1, o2, o3);
    loader_steps<T + 1, nxt>(src, dst0, o0, o1, o2, o3);
  }
}
template <int I> __device__ __forceinline__ void tail_waits() {
  if constexpr (I >= 0) { wait_bar<8 * I>(); tail_waits<I - 1>(); }
}

__global__ __launch_bounds__(384, 2) void gqa_kernel(const __hip_bfloat16* __restrict__ Qp, const __hip_bfloat16* __restrict__ Kp,
                                                      const __hip_bfloat16* __restrict__ Vp, __hip_bfloat16* __restrict__ Op, char* __restrict__ ws) {
  extern __shared__ __attribute__((aligned(16))) char smem[];
  const int tid = threadIdx.x, lane = tid & 63;
  const int wv = __builtin_amdgcn_readfirstlane(tid >> 6);
  const int bid = blockIdx.x, xcd = bid & 7, idx = bid >> 3;
  const int unit = xcd * 8 + (idx >> 2), g = idx & 3;
  const unsigned smem_base = (unsigned)(uintptr_t)smem;
  unsigned long long rt0 = __builtin_amdgcn_s_memrealtime();

  if (__builtin_expect(wv < 2, 1)) {
    const int li = lane & 15, lq = lane >> 4;
    const unsigned sw = wv == 0 ? lq : (lq << 2);
    const unsigned o0 = lq * 256 + ((li ^ sw) * 16);
    const unsigned x = wv == 0 ? 64 : 0;
    const unsigned o1 = o0 ^ x, o2 = o0 ^ (2 * x), o3 = o0 ^ (3 * x);
    const char* src = (wv == 0 ? (const char*)Kp : (const char*)Vp) + ((size_t)unit * SK + (size_t)g * KPW) * D * 2;
    const unsigned dst0 = smem_base + wv * TILE_B;
#define P0 LA
#pragma unroll
    for (int t = 0; t < P0; ++t) dma8(src + (size_t)t * TILE_B, dst0 + t * 2 * TILE_B, o0, o1, o2, o3);
    loader_steps<0, P0>(src, dst0, o0, o1, o2, o3);
    asm volatile("s_barrier" ::: "memory");
    return;
  }
  const int wid = wv - 2;
  const int r32 = lane & 31, h = lane >> 5;
  bf16x8 qf[8];
  {
    const char* Qb = (const char*)Qp + (size_t)(unit * 4 + wid) * SQ * D * 2 + r32 * 256 + h * 16;
#pragma unroll
    for (int ks = 0; ks < 8; ++ks) {
      const u32x4 raw = *(const u32x4*)(Qb + ks * 32);
      u32x4 w;
#pragma unroll
      for (int e = 0; e < 4; ++e)
        w[e] = pk_bf16(__uint_as_float(raw[e] << 16) * SC, __uint_as_float(raw[e] & 0xffff0000u) * SC);
      qf[ks] = __builtin_bit_cast(bf16x8, w);
    }
  }
  const unsigned long long tag_pub = *(const unsigned long long*)(ws + WS_E + (unit * 4 + wid) * 8) + 1;
  const unsigned long long tag_own = *(const unsigned long long*)(ws + WS_E + (unit * 4 + g) * 8) + 1;

  const unsigned kr = smem_base + r32 * 256 + ((h ^ (r32 & 15)) * 16);
  const int tq = (lane & 15) >> 2, tp = lane & 3, tblk = (lane >> 4) & 1;
  const unsigned vr = smem_base + TILE_B + 256 * (4 * h + tq) + 32 * tblk + 8 * tp;

  f32x16 o[4];
#pragma unroll
  for (int i = 0; i < 4; ++i)
#pragma unroll
    for (int j = 0; j < 16; ++j) o[i][j] = 0.f;
  float m, l = 0.f;
  f32x16 sa, sb, negm;
  bf16x8 kf[8];
  s16x4 vlo[8], vhi[8];
  unsigned ka[8], va[4];
#pragma unroll
  for (int ks = 0; ks < 8; ++ks) ka[ks] = kr ^ (32 * ks);
#pragma unroll
  for (int db = 0; db < 4; ++db) va[db] = vr + 64 * (db ^ tq);
#define SB() __builtin_amdgcn_sched_barrier(0)
#define PIN(v) asm volatile("" : "+v"(v))
#define KREAD(ks, tile) kf[ks] = *(lds_bf16x8_p)(uintptr_t)(ka[ks] + ((tile) & 3) * 2 * TILE_B)
#define VREAD(i, tile)                                                                                                                         \
  do {                                                                                                                                         \
    vlo[i] = __builtin_amdgcn_ds_read_tr16_b64_v4i16((lds_s16x4_p)(uintptr_t)(va[(i) & 3] + 4096 * ((i) >> 2) + ((tile) & 3) * 2 * TILE_B));        \
    vhi[i] = __builtin_amdgcn_ds_read_tr16_b64_v4i16((lds_s16x4_p)(uintptr_t)(va[(i) & 3] + 4096 * ((i) >> 2) + 2048 + ((tile) & 3) * 2 * TILE_B)); \
  } while (0)
  {
    const f32x16 zero = {};
    asm volatile("s_barrier" ::: "memory");
#pragma unroll
    for (int ks = 0; ks < 8; ++ks) KREAD(ks, 0);
    sa = __builtin_amdgcn_mfma_f32_32x32x16_bf16(kf[0], qf[0], zero, 0, 0, 0);
#pragma unroll
    for (int ks = 1; ks < 8; ++ks) sa = __builtin_amdgcn_mfma_f32_32x32x16_bf16(kf[ks], qf[ks], sa, 0, 0, 0);
    SB();
    asm volatile("s_barrier" ::: "memory");
#pragma unroll
    for (int ks = 0; ks < 8; ++ks) KREAD(ks, 1);
    float mx = sa[0];
#pragma unroll
    for (int j = 1; j < 16; ++j) mx = fmaxf(mx, sa[j]);
    auto rr = __builtin_amdgcn_permlane32_swap(__float_as_uint(mx), __float_as_uint(mx), false, false);
    m = fmaxf(__uint_as_float(rr[0]), __uint_as_float(rr[1]));
#pragma unroll
    for (int j = 0; j < 16; ++j) { sa[j] -= m; negm[j] = -m; }
    SB();
  }
#define ITER(t, S, X)                                                                                              \
  {                                                                                                                \
    unsigned pw[8];                                                                                                \
    _Pragma("unroll") for (int k = 0; k < 8; ++k) {                                                                \
      if (k == 0) S = __builtin_amdgcn_mfma_f32_32x32x16_bf16(kf[0], qf[0], negm, 0, 0, 0);                        \
      else S = __builtin_amdgcn_mfma_f32_32x32x16_bf16(kf[k], qf[k], S, 0, 0, 0);                                  \
      SB();                                                                                                        \
      VREAD(k, (t) - 1);                                                                                           \
      X[2 * k] = __builtin_amdgcn_exp2f(X[2 * k]);                                                                 \
      X[2 * k + 1] = __builtin_amdgcn_exp2f(X[2 * k + 1]);                                                         \
      if (k > 0) {                                                                                                 \
        pw[k - 1] = pk_bf16(X[2 * k - 2], X[2 * k - 1]);                                                           \
        if (k & 1) { l += X[2 * k - 2]; PIN(l); }                                                                  \
      }                                                                                                            \
      SB();                                                                                                        \
    }                                                                                                              \
    pw[7] = pk_bf16(X[14], X[15]);                                                                                 \
    asm volatile("s_barrier" ::: "memory");                     \
    const bf16x8 pf0 = __builtin_bit_cast(bf16x8, u32x4{pw[0], pw[1], pw[2], pw[3]});                              \
    const bf16x8 pf1 = __builtin_bit_cast(bf16x8, u32x4{pw[4], pw[5], pw[6], pw[7]});                              \
    float mx = 0.f;                                                                                                \
    SB();                                                                                                          \
    _Pragma("unroll") for (int k = 0; k < 8; ++k) {                                                                \
      const bf16x8 vf = __builtin_shufflevector(vlo[k], vhi[k], 0, 1, 2, 3, 4, 5, 6, 7);                           \
      o[k & 3] = __builtin_amdgcn_mfma_f32_32x32x16_bf16(vf, k < 4 ? pf0 : pf1, o[k & 3], 0, 0, 0);                \
      SB();                                                                                                        \
      if ((((t) + 1) & 3) == 0) { ka[k] += ((((t) + 1) & 4) ? 8 * TILE_B : -8 * TILE_B); PIN(ka[k]); }                                               \
      if ((t) + 1 < NT) KREAD(k, (t) + 1);                                                                         \
      if ((((t)) & 3) == 0 && k < 4) { va[k] += (((t) & 4) ? 8 * TILE_B : -8 * TILE_B); PIN(va[k]); }                                          \
                                             \
      l += X[2 * k + 1];                                                                                           \
      if (k & 1) l += X[2 * k];                                                                                    \
      PIN(l);                                                                                                      \
      if (k == 2) { PIN(S); mx = fmaxf(fmaxf(S[0], S[1]), S[2]); mx = fmaxf(fmaxf(mx, S[3]), S[4]); PIN(mx); }     \
      if (k == 3) { mx = fmaxf(fmaxf(mx, S[5]), S[6]); mx = fmaxf(fmaxf(mx, S[7]), S[8]); PIN(mx); }               \
      if (k == 4) { mx = fmaxf(fmaxf(mx, S[9]), S[10]); mx = fmaxf(fmaxf(mx, S[11]), S[12]); PIN(mx); }            \
      if (k == 5) { mx = fmaxf(fmaxf(mx, S[13]), S[14]); mx = fmaxf(mx, S[15]); PIN(mx); }                         \
      if (k == 6) {                                                                                                \
        auto rr = __builtin_amdgcn_permlane32_swap(__float_as_uint(mx), __float_as_uint(mx), false, false);        \
        mx = fmaxf(__uint_as_float(rr[0]), __uint_as_float(rr[1]));                                                \
      }                                                                                                            \
      SB();                                                                                                        \
    }                                                                                                              \
    if (RESCALE_COND(mx)) {   \
      const float d = fmaxf(mx, 0.f);                                                                              \
      const float alpha = __builtin_amdgcn_exp2f(-d);                                                              \
      m += d;                                                                                                      \
      l *= alpha;                                                                                                  \
      _Pragma("unroll") for (int j = 0; j < 16; ++j) { S[j] -= d; negm[j] = -m; }                                  \
      _Pragma("unroll") for (int i = 0; i < 4; ++i) _Pragma("unroll") for (int j = 0; j < 16; ++j) o[i][j] *= alpha; \
    }                                                                                                              \
    SB();                                                                                                          \
  }
#define RESCALE_COND(mx) (__any(!(mx <= THR)))
  ITER(1, sb, sa) ITER(2, sa, sb) ITER(3, sb, sa) ITER(4, sa, sb) ITER(5, sb, sa) ITER(6, sa, sb) ITER(7, sb, sa) ITER(8, sa, sb)
  ITER(9, sb, sa) ITER(10, sa, sb) ITER(11, sb, sa) ITER(12, sa, sb) ITER(13, sb, sa) ITER(14, sa, sb) ITER(15, sb, sa)
  {
#pragma unroll
    for (int k = 0; k < 8; ++k) VREAD(k, NT - 1);
#pragma unroll
    for (int j = 0; j < 16; ++j) sb[j] = __builtin_amdgcn_exp2f(sb[j]);
    const bf16x8 pf0 = __builtin_bit_cast(bf16x8, u32x4{pk_bf16(sb[0], sb[1]), pk_bf16(sb[2], sb[3]), pk_bf16(sb[4], sb[5]), pk_bf16(sb[6], sb[7])});
    const bf16x8 pf1 = __builtin_bit_cast(bf16x8, u32x4{pk_bf16(sb[8], sb[9]), pk_bf16(sb[10], sb[11]), pk_bf16(sb[12], sb[13]), pk_bf16(sb[14], sb[15])});
#pragma unroll
    for (int k = 0; k < 8; ++k) {
      const bf16x8 vf = __builtin_shufflevector(vlo[k], vhi[k], 0, 1, 2, 3, 4, 5, 6, 7);
      o[k & 3] = __builtin_amdgcn_mfma_f32_32x32x16_bf16(vf, k < 4 ? pf0 : pf1, o[k & 3], 0, 0, 0);
    }
#pragma unroll
    for (int j = 0; j < 16; ++j) l += sb[j];
  }

  asm volatile("" :: "v"(o[0][0]), "v"(o[1][0]), "v"(o[2][0]), "v"(o[3][0]));
  unsigned long long r1 = __builtin_amdgcn_s_memrealtime();
  {
    auto rr = __builtin_amdgcn_permlane32_swap(__float_as_uint(l), __float_as_uint(l), false, false);
    l = __uint_as_float(rr[0]) + __uint_as_float(rr[1]);
  }
  const auto rsrc = __builtin_amdgcn_make_buffer_rsrc((void*)ws, 0, (int)WS_END, 0x00020000);
  const unsigned xo = smem_base;
  const unsigned xml = smem_base + 16384;
  if (wid != g) {
    const int slab = (unit * 4 + wid) * 4 + g;
    const unsigned ob = (unsigned)(WS_O + (size_t)slab * 8192) + lane * 16;
#pragma unroll
    for (int db = 0; db < 4; ++db)
#pragma unroll
      for (int hh = 0; hh < 2; ++hh) {
        u32x4 v = {pk_bf16(o[db][8 * hh], o[db][8 * hh + 1]), pk_bf16(o[db][8 * hh + 2], o[db][8 * hh + 3]), pk_bf16(o[db][8 * hh + 4], o[db][8 * hh + 5]),
                   pk_bf16(o[db][8 * hh + 6], o[db][8 * hh + 7])};
        __builtin_amdgcn_raw_buffer_store_b128(v, rsrc, ob + (db * 2 + hh) * 1024, 0, 16);
      }
    if (h == 0) {
      u32x2 v = {__float_as_uint(m), __float_as_uint(l)};
      __builtin_amdgcn_raw_buffer_store_b64(v, rsrc, (unsigned)(WS_ML + slab * 256) + r32 * 8, 0, 16);
    }
    asm volatile("s_waitcnt vmcnt(0)" ::: "memory");
    if (lane == 0) __hip_atomic_store((gu64*)(ws + WS_F + slab * 8), tag_pub, __ATOMIC_RELAXED, __HIP_MEMORY_SCOPE_AGENT);
  } else {
#pragma unroll
    for (int db = 0; db < 4; ++db)
#pragma unroll
      for (int gq = 0; gq < 4; ++gq) {
        f32x4 v = {o[db][4 * gq], o[db][4 * gq + 1], o[db][4 * gq + 2], o[db][4 * gq + 3]};
        *(lds_f32x4_p)(uintptr_t)(xo + (db * 4 + gq) * 1024 + lane * 16) = v;
      }
    if (h == 0) {
      f32x2 v = {m, l};
      *(lds_f32x2_p)(uintptr_t)(xml + r32 * 8) = v;
    }
  }
  __syncthreads();
  unsigned long long r2 = __builtin_amdgcn_s_memrealtime();
  {
    const gu64* fp = (const gu64*)(ws + WS_F + (size_t)((unit * 4 + g) * 4) * 8);
    const int sl = lane & 3;
#pragma unroll 1
    for (unsigned spins = 0; spins < 20000000u; ++spins) {
      const unsigned long long v = __hip_atomic_load(fp + sl, __ATOMIC_RELAXED, __HIP_MEMORY_SCOPE_AGENT);
      const bool ok = (sl == g) || (v == tag_own);
      if (__all(ok)) break;
      __builtin_amdgcn_s_sleep(1);
    }
    __builtin_amdgcn_fence(__ATOMIC_ACQUIRE, "wavefront");
  }
  unsigned long long r3 = __builtin_amdgcn_s_memrealtime();
  {
    f32x2 ml0 = *(lds_f32x2_p)(uintptr_t)(xml + r32 * 8);
    u32x4 ro[3][2];
    u32x2 rml[3];
#pragma unroll
    for (int i = 0; i < 3; ++i) {
      const int sg = (g + 1 + i) & 3;
      const int slab = (unit * 4 + g) * 4 + sg;
      rml[i] = __builtin_amdgcn_raw_buffer_load_b64(rsrc, (unsigned)(WS_ML + slab * 256) + r32 * 8, 0, 16);
#pragma unroll
      for (int hh = 0; hh < 2; ++hh)
        ro[i][hh] = __builtin_amdgcn_raw_buffer_load_b128(rsrc, (unsigned)(WS_O + (size_t)slab * 8192) + lane * 16 + (wid * 2 + hh) * 1024, 0, 16);
    }
    f32x4 own[4];
#pragma unroll
    for (int gq = 0; gq < 4; ++gq) own[gq] = *(lds_f32x4_p)(uintptr_t)(xo + (wid * 4 + gq) * 1024 + lane * 16);
    float M = ml0[0];
#pragma unroll
    for (int i = 0; i < 3; ++i) M = fmaxf(M, __uint_as_float(rml[i][0]));
    const float f0 = __builtin_amdgcn_exp2f(ml0[0] - M);
    float L = f0 * ml0[1];
    float f[3];
#pragma unroll
    for (int i = 0; i < 3; ++i) {
      f[i] = __builtin_amdgcn_exp2f(__uint_as_float(rml[i][0]) - M);
      L += f[i] * __uint_as_float(rml[i][1]);
    }
    const float inv = 1.f / L;
    char* orow = (char*)Op + ((size_t)(unit * 4 + g) * SQ + r32) * D * 2 + wid * 64 + h * 8;
#pragma unroll
    for (int gq = 0; gq < 4; ++gq) {
      float a[4];
#pragma unroll
      for (int e = 0; e < 4; ++e) {
        float x = f0 * own[gq][e];
#pragma unroll
        for (int i = 0; i < 3; ++i) {
          const unsigned wd = ro[i][gq >> 1][(gq & 1) * 2 + (e >> 1)];
          x += f[i] * __uint_as_float((e & 1) ? (wd & 0xffff0000u) : (wd << 16));
        }
        a[e] = x * inv;
      }
      u32x2 w = {pk_bf16(a[0], a[1]), pk_bf16(a[2], a[3])};
      *(u32x2*)(orow + gq * 16) = w;
    }
  }
  {
    asm volatile("s_waitcnt vmcnt(0)" ::: "memory");
    unsigned long long r4 = __builtin_amdgcn_s_memrealtime();
    if (bid == PROBE_BID && wid == 0) {
      unsigned long long d = PROBE == 1 ? 10 * (r1 - rt0) : PROBE == 2 ? 10 * (r2 - r1) : PROBE == 3 ? 10 * (r3 - r2) : PROBE == 4 ? 10 * (r4 - r3) : 0;
      while (__builtin_amdgcn_s_memrealtime() < r4 + d) __builtin_amdgcn_s_sleep(2);
    }
  }
  __syncthreads();
  if (tid == 128) *(unsigned long long*)(ws + WS_E + (unit * 4 + g) * 8) = tag_own;
}

extern "C" void kernel_launch(void* const* d_in, const int* in_sizes, int n_in, void* d_out, int out_size, void* d_ws, size_t ws_size,
                              hipStream_t stream) {
  (void)in_sizes; (void)n_in; (void)out_size; (void)ws_size;
  (void)hipFuncSetAttribute((const void*)gqa_kernel, hipFuncAttributeMaxDynamicSharedMemorySize, LDS_BYTES);
  hipLaunchKernelGGL(gqa_kernel, dim3(256), dim3(384), LDS_BYTES, stream, (const __hip_bfloat16*)d_in[0], (const __hip_bfloat16*)d_in[1],
                     (const __hip_bfloat16*)d_in[2], (__hip_bfloat16*)d_out, (char*)d_ws);
}
```

```cpp
#define PROBE 0
#define PROBE_BID 100
#define WBALL (bid < 8)
#include <hip/hip_runtime.h>
#include <hip/hip_bf16.h>

typedef short bf16x8 __attribute__((ext_vector_type(8)));
typedef short s16x4 __attribute__((ext_vector_type(4)));
typedef float f32x16 __attribute__((ext_vector_type(16)));
typedef float f32x4 __attribute__((ext_vector_type(4)));
typedef float f32x2 __attribute__((ext_vector_type(2)));
typedef __bf16 bf16x2_t __attribute__((ext_vector_type(2)));
typedef unsigned u32x4 __attribute__((ext_vector_type(4)));
typedef unsigned u32x2 __attribute__((ext_vector_type(2)));
typedef __attribute__((address_space(3))) s16x4* lds_s16x4_p;
typedef __attribute__((address_space(3))) bf16x8* lds_bf16x8_p;
typedef __attribute__((address_space(3))) f32x4* lds_f32x4_p;
typedef __attribute__((address_space(3))) f32x2* lds_f32x2_p;
typedef __attribute__((address_space(1))) unsigned long long gu64;

constexpr int SQ = 32, SK = 2048, D = 128;
constexpr int NSPLIT = 4, KT = 32, KPW = SK / NSPLIT, NT = KPW / KT;
constexpr int NS = 8;
#define LA 4
static_assert(LA <= NS - 3, "lookahead");
constexpr int TILE_B = KT * D * 2;
constexpr int LDS_BYTES = NS * 2 * TILE_B;
constexpr float SC = 0.088388347648318447f * 1.4426950408889634f;
constexpr float THR = 8.f;
constexpr size_t WS_E = 0, WS_F = 4096, WS_ML = 65536, WS_O = 1 << 20;
constexpr size_t WS_END = WS_O + (size_t)64 * 4 * 4 * 16384;

__device__ __forceinline__ unsigned pk_bf16(float a, float b) {
  f32x2 v = {a, b};
  bf16x2_t r = __builtin_convertvector(v, bf16x2_t);
  return __builtin_bit_cast(unsigned, r);
}

__device__ __forceinline__ void dma8(const char* gbase, unsigned lds, unsigned o0, unsigned o1, unsigned o2, unsigned o3) {
  unsigned keep;
  const char* g2 = gbase + 4096;
  const unsigned lds2 = lds + 4096;
  asm volatile(
      "s_mov_b32 %0, m0\n\t"
      "s_mov_b32 m0, %7\n\t"
      "s_nop 0\n\t"
      "global_load_lds_dwordx4 %1, %5 offset:0 nt\n\t"
      "global_load_lds_dwordx4 %2, %5 offset:1024 nt\n\t"
      "global_load_lds_dwordx4 %3, %5 offset:2048 nt\n\t"
      "global_load_lds_dwordx4 %4, %5 offset:3072 nt\n\t"
      "s_mov_b32 m0, %8\n\t"
      "s_nop 0\n\t"
      "global_load_lds_dwordx4 %1, %6 offset:0 nt\n\t"
      "global_load_lds_dwordx4 %2, %6 offset:1024 nt\n\t"
      "global_load_lds_dwordx4 %3, %6 offset:2048 nt\n\t"
      "global_load_lds_dwordx4 %4, %6 offset:3072 nt\n\t"
      "s_mov_b32 m0, %0"
      : "=&s"(keep)
      : "v"(o0), "v"(o1), "v"(o2), "v"(o3), "s"(gbase), "s"(g2), "s"(lds), "s"(lds2)
      : "memory");
}

template <int N> __device__ __forceinline__ void wait_bar() { asm volatile("s_waitcnt vmcnt(%0)\n\ts_barrier" ::"i"(N) : "memory"); }
#define RAMP 2
template <int T, int ISS> __device__ __forceinline__ void loader_steps(const char* src, unsigned dst0, unsigned o0, unsigned o1, unsigned o2, unsigned o3) {
  if constexpr (T < NT) {
    wait_bar<8 * (ISS - T - 1)>();
    constexpr int want = T + 1 + LA < NT ? T + 1 + LA : NT;
    constexpr int nxt = ISS + RAMP < want ? ISS + RAMP : (want > ISS ? want : ISS);
#pragma unroll
    for (int t = ISS; t < nxt; ++t) dma8(src + (size_t)t * TILE_B, dst0 + (t % NS) * 2 * TILE_B, o0, o1, o2, o3);
    loader_steps<T + 1, nxt>(src, dst0, o0, o1, o2, o3);
  }
}
template <int I> __device__ __forceinline__ void tail_waits() {
  if constexpr (I >= 0) { wait_bar<8 * I>(); tail_waits<I - 1>(); }
}

__global__ __launch_bounds__(384, 2) void gqa_kernel(const __hip_bfloat16* __restrict__ Qp, const __hip_bfloat16* __restrict__ Kp,
                                                      const __hip_bfloat16* __restrict__ Vp, __hip_bfloat16* __restrict__ Op, char* __restrict__ ws) {
  extern __shared__ __attribute__((aligned(16))) char smem[];
  const int tid = threadIdx.x, lane = tid & 63;
  const int wv = __builtin_amdgcn_readfirstlane(tid >> 6);
  const int bid = blockIdx.x, xcd = bid & 7, idx = bid >> 3;
  const int unit = xcd * 8 + (idx >> 2), g = idx & 3;
  const unsigned smem_base = (unsigned)(uintptr_t)smem;
  unsigned long long rt0 = __builtin_amdgcn_s_memrealtime();

  if (__builtin_expect(wv < 2, 1)) {
    const int li = lane & 15, lq = lane >> 4;
    const unsigned sw = wv == 0 ? lq : (lq << 2);
    const unsigned o0 = lq * 256 + ((li ^ sw) * 16);
    const unsigned x = wv == 0 ? 64 : 0;
    const unsigned o1 = o0 ^ x, o2 = o0 ^ (2 * x), o3 = o0 ^ (3 * x);
    const char* src = (wv == 0 ? (const char*)Kp : (const char*)Vp) + ((size_t)unit * SK + (size_t)g * KPW) * D * 2;
    const unsigned dst0 = smem_base + wv * TILE_B;
#define P0 LA
#pragma unroll
    for (int t = 0; t < P0; ++t) dma8(src + (size_t)t * TILE_B, dst0 + t * 2 * TILE_B, o0, o1, o2, o3);
    loader_steps<0, P0>(src, dst0, o0, o1, o2, o3);
    asm volatile("s_barrier" ::: "memory");
    return;
  }
  const int wid = wv - 2;
  unsigned long long w0 = __builtin_amdgcn_s_memrealtime(), w1 = w0;
  if (wid == 0 && (WBALL || bid == PROBE_BID)) {
    asm volatile("buffer_wbl2 sc1\n\ts_waitcnt vmcnt(0)" ::: "memory");
    w1 = __builtin_amdgcn_s_memrealtime();
  }
  const int r32 = lane & 31, h = lane >> 5;
  bf16x8 qf[8];
  {
    const char* Qb = (const char*)Qp + (size_t)(unit * 4 + wid) * SQ * D * 2 + r32 * 256 + h * 16;
#pragma unroll
    for (int ks = 0; ks < 8; ++ks) {
      const u32x4 raw = *(const u32x4*)(Qb + ks * 32);
      u32x4 w;
#pragma unroll
      for (int e = 0; e < 4; ++e)
        w[e] = pk_bf16(__uint_as_float(raw[e] << 16) * SC, __uint_as_float(raw[e] & 0xffff0000u) * SC);
      qf[ks] = __builtin_bit_cast(bf16x8, w);
    }
  }
  const unsigned long long tag_pub = *(const unsigned long long*)(ws + WS_E + (unit * 4 + wid) * 8) + 1;
  const unsigned long long tag_own = *(const unsigned long long*)(ws + WS_E + (unit * 4 + g) * 8) + 1;

  const unsigned kr = smem_base + r32 * 256 + ((h ^ (r32 & 15)) * 16);
  const int tq = (lane & 15) >> 2, tp = lane & 3, tblk = (lane >> 4) & 1;
  const unsigned vr = smem_base + TILE_B + 256 * (4 * h + tq) + 32 * tblk + 8 * tp;

  f32x16 o[4];
#pragma unroll
  for (int i = 0; i < 4; ++i)
#pragma unroll
    for (int j = 0; j < 16; ++j) o[i][j] = 0.f;
  float m, l = 0.f;
  f32x16 sa, sb, negm;
  bf16x8 kf[8];
  s16x4 vlo[8], vhi[8];
  unsigned ka[8], va[4];
#pragma unroll
  for (int ks = 0; ks < 8; ++ks) ka[ks] = kr ^ (32 * ks);
#pragma unroll
  for (int db = 0; db < 4; ++db) va[db] = vr + 64 * (db ^ tq);
#define SB() __builtin_amdgcn_sched_barrier(0)
#define PIN(v) asm volatile("" : "+v"(v))
#define KREAD(ks, tile) kf[ks] = *(lds_bf16x8_p)(uintptr_t)(ka[ks] + ((tile) & 3) * 2 * TILE_B)
#define VREAD(i, tile)                                                                                                                         \
  do {                                                                                                                                         \
    vlo[i] = __builtin_amdgcn_ds_read_tr16_b64_v4i16((lds_s16x4_p)(uintptr_t)(va[(i) & 3] + 4096 * ((i) >> 2) + ((tile) & 3) * 2 * TILE_B));        \
    vhi[i] = __builtin_amdgcn_ds_read_tr16_b64_v4i16((lds_s16x4_p)(uintptr_t)(va[(i) & 3] + 4096 * ((i) >> 2) + 2048 + ((tile) & 3) * 2 * TILE_B)); \
  } while (0)
  {
    const f32x16 zero = {};
    asm volatile("s_barrier" ::: "memory");
#pragma unroll
    for (int ks = 0; ks < 8; ++ks) KREAD(ks, 0);
    sa = __builtin_amdgcn_mfma_f32_32x32x16_bf16(kf[0], qf[0], zero, 0, 0, 0);
#pragma unroll
    for (int ks = 1; ks < 8; ++ks) sa = __builtin_amdgcn_mfma_f32_32x32x16_bf16(kf[ks], qf[ks], sa, 0, 0, 0);
    SB();
    asm volatile("s_barrier" ::: "memory");
#pragma unroll
    for (int ks = 0; ks < 8; ++ks) KREAD(ks, 1);
    float mx = sa[0];
#pragma unroll
    for (int j = 1; j < 16; ++j) mx = fmaxf(mx, sa[j]);
    auto rr = __builtin_amdgcn_permlane32_swap(__float_as_uint(mx), __float_as_uint(mx), false, false);
    m = fmaxf(__uint_as_float(rr[0]), __uint_as_float(rr[1]));
#pragma unroll
    for (int j = 0; j < 16; ++j) { sa[j] -= m; negm[j] = -m; }
    SB();
  }
#define ITER(t, S, X)                                                                                              \
  {                                                                                                                \
    unsigned pw[8];                                                                                                \
    _Pragma("unroll") for (int k = 0; k < 8; ++k) {                                                                \
      if (k == 0) S = __builtin_amdgcn_mfma_f32_32x32x16_bf16(kf[0], qf[0], negm, 0, 0, 0);                        \
      else S = __builtin_amdgcn_mfma_f32_32x32x16_bf16(kf[k], qf[k], S, 0, 0, 0);                                  \
      SB();                                                                                                        \
      VREAD(k, (t) - 1);                                                                                           \
      X[2 * k] = __builtin_amdgcn_exp2f(X[2 * k]);                                                                 \
      X[2 * k + 1] = __builtin_amdgcn_exp2f(X[2 * k + 1]);                                                         \
      if (k > 0) {                                                                                                 \
        pw[k - 1] = pk_bf16(X[2 * k - 2], X[2 * k - 1]);                                                           \
        if (k & 1) { l += X[2 * k - 2]; PIN(l); }                                                                  \
      }                                                                                                            \
      SB();                                                                                                        \
    }                                                                                                              \
    pw[7] = pk_bf16(X[14], X[15]);                                                                                 \
    asm volatile("s_barrier" ::: "memory");                     \
    const bf16x8 pf0 = __builtin_bit_cast(bf16x8, u32x4{pw[0], pw[1], pw[2], pw[3]});                              \
    const bf16x8 pf1 = __builtin_bit_cast(bf16x8, u32x4{pw[4], pw[5], pw[6], pw[7]});                              \
    float mx = 0.f;                                                                                                \
    SB();                                                                                                          \
    _Pragma("unroll") for (int k = 0; k < 8; ++k) {                                                                \
      const bf16x8 vf = __builtin_shufflevector(vlo[k], vhi[k], 0, 1, 2, 3, 4, 5, 6, 7);                           \
      o[k & 3] = __builtin_amdgcn_mfma_f32_32x32x16_bf16(vf, k < 4 ? pf0 : pf1, o[k & 3], 0, 0, 0);                \
      SB();                                                                                                        \
      if ((((t) + 1) & 3) == 0) { ka[k] += ((((t) + 1) & 4) ? 8 * TILE_B : -8 * TILE_B); PIN(ka[k]); }                                               \
      if ((t) + 1 < NT) KREAD(k, (t) + 1);                                                                         \
      if ((((t)) & 3) == 0 && k < 4) { va[k] += (((t) & 4) ? 8 * TILE_B : -8 * TILE_B); PIN(va[k]); }                                          \
                                             \
      l += X[2 * k + 1];                                                                                           \
      if (k & 1) l += X[2 * k];                                                                                    \
      PIN(l);                                                                                                      \
      if (k == 2) { PIN(S); mx = fmaxf(fmaxf(S[0], S[1]), S[2]); mx = fmaxf(fmaxf(mx, S[3]), S[4]); PIN(mx); }     \
      if (k == 3) { mx = fmaxf(fmaxf(mx, S[5]), S[6]); mx = fmaxf(fmaxf(mx, S[7]), S[8]); PIN(mx); }               \
      if (k == 4) { mx = fmaxf(fmaxf(mx, S[9]), S[10]); mx = fmaxf(fmaxf(mx, S[11]), S[12]); PIN(mx); }            \
      if (k == 5) { mx = fmaxf(fmaxf(mx, S[13]), S[14]); mx = fmaxf(mx, S[15]); PIN(mx); }                         \
      if (k == 6) {                                                                                                \
        auto rr = __builtin_amdgcn_permlane32_swap(__float_as_uint(mx), __float_as_uint(mx), false, false);        \
        mx = fmaxf(__uint_as_float(rr[0]), __uint_as_float(rr[1]));                                                \
      }                                                                                                            \
      SB();                                                                                                        \
    }                                                                                                              \
    if (RESCALE_COND(mx)) {   \
      const float d = fmaxf(mx, 0.f);                                                                              \
      const float alpha = __builtin_amdgcn_exp2f(-d);                                                              \
      m += d;                                                                                                      \
      l *= alpha;                                                                                                  \
      _Pragma("unroll") for (int j = 0; j < 16; ++j) { S[j] -= d; negm[j] = -m; }                                  \
      _Pragma("unroll") for (int i = 0; i < 4; ++i) _Pragma("unroll") for (int j = 0; j < 16; ++j) o[i][j] *= alpha; \
    }                                                                                                              \
    SB();                                                                                                          \
  }
#define RESCALE_COND(mx) (__any(!(mx <= THR)))
  ITER(1, sb, sa) ITER(2, sa, sb) ITER(3, sb, sa) ITER(4, sa, sb) ITER(5, sb, sa) ITER(6, sa, sb) ITER(7, sb, sa) ITER(8, sa, sb)
  ITER(9, sb, sa) ITER(10, sa, sb) ITER(11, sb, sa) ITER(12, sa, sb) ITER(13, sb, sa) ITER(14, sa, sb) ITER(15, sb, sa)
  {
#pragma unroll
    for (int k = 0; k < 8; ++k) VREAD(k, NT - 1);
#pragma unroll
    for (int j = 0; j < 16; ++j) sb[j] = __builtin_amdgcn_exp2f(sb[j]);
    const bf16x8 pf0 = __builtin_bit_cast(bf16x8, u32x4{pk_bf16(sb[0], sb[1]), pk_bf16(sb[2], sb[3]), pk_bf16(sb[4], sb[5]), pk_bf16(sb[6], sb[7])});
    const bf16x8 pf1 = __builtin_bit_cast(bf16x8, u32x4{pk_bf16(sb[8], sb[9]), pk_bf16(sb[10], sb[11]), pk_bf16(sb[12], sb[13]), pk_bf16(sb[14], sb[15])});
#pragma unroll
    for (int k = 0; k < 8; ++k) {
      const bf16x8 vf = __builtin_shufflevector(vlo[k], vhi[k], 0, 1, 2, 3, 4, 5, 6, 7);
      o[k & 3] = __builtin_amdgcn_mfma_f32_32x32x16_bf16(vf, k < 4 ? pf0 : pf1, o[k & 3], 0, 0, 0);
    }
#pragma unroll
    for (int j = 0; j < 16; ++j) l += sb[j];
  }

  asm volatile("" :: "v"(o[0][0]), "v"(o[1][0]), "v"(o[2][0]), "v"(o[3][0]));
  unsigned long long r1 = __builtin_amdgcn_s_memrealtime();
  {
    auto rr = __builtin_amdgcn_permlane32_swap(__float_as_uint(l), __float_as_uint(l), false, false);
    l = __uint_as_float(rr[0]) + __uint_as_float(rr[1]);
  }
  const auto rsrc = __builtin_amdgcn_make_buffer_rsrc((void*)ws, 0, (int)WS_END, 0x00020000);
  const unsigned xo = smem_base;
  const unsigned xml = smem_base + 16384;
  if (wid != g) {
    const int slab = (unit * 4 + wid) * 4 + g;
    const unsigned ob = (unsigned)(WS_O + (size_t)slab * 8192) + lane * 16;
#pragma unroll
    for (int db = 0; db < 4; ++db)
#pragma unroll
      for (int hh = 0; hh < 2; ++hh) {
        u32x4 v = {pk_bf16(o[db][8 * hh], o[db][8 * hh + 1]), pk_bf16(o[db][8 * hh + 2], o[db][8 * hh + 3]), pk_bf16(o[db][8 * hh + 4], o[db][8 * hh + 5]),
                   pk_bf16(o[db][8 * hh + 6], o[db][8 * hh + 7])};
        __builtin_amdgcn_raw_buffer_store_b128(v, rsrc, ob + (db * 2 + hh) * 1024, 0, 16);
      }
    if (h == 0) {
      u32x2 v = {__float_as_uint(m), __float_as_uint(l)};
      __builtin_amdgcn_raw_buffer_store_b64(v, rsrc, (unsigned)(WS_ML + slab * 256) + r32 * 8, 0, 16);
    }
    asm volatile("s_waitcnt vmcnt(0)" ::: "memory");
    if (lane == 0) __hip_atomic_store((gu64*)(ws + WS_F + slab * 8), tag_pub, __ATOMIC_RELAXED, __HIP_MEMORY_SCOPE_AGENT);
  } else {
#pragma unroll
    for (int db = 0; db < 4; ++db)
#pragma unroll
      for (int gq = 0; gq < 4; ++gq) {
        f32x4 v = {o[db][4 * gq], o[db][4 * gq + 1], o[db][4 * gq + 2], o[db][4 * gq + 3]};
        *(lds_f32x4_p)(uintptr_t)(xo + (db * 4 + gq) * 1024 + lane * 16) = v;
      }
    if (h == 0) {
      f32x2 v = {m, l};
      *(lds_f32x2_p)(uintptr_t)(xml + r32 * 8) = v;
    }
  }
  __syncthreads();
  unsigned long long r2 = __builtin_amdgcn_s_memrealtime();
  {
    const gu64* fp = (const gu64*)(ws + WS_F + (size_t)((unit * 4 + g) * 4) * 8);
    const int sl = lane & 3;
#pragma unroll 1
    for (unsigned spins = 0; spins < 20000000u; ++spins) {
      const unsigned long long v = __hip_atomic_load(fp + sl, __ATOMIC_RELAXED, __HIP_MEMORY_SCOPE_AGENT);
      const bool ok = (sl == g) || (v == tag_own);
      if (__all(ok)) break;
      __builtin_amdgcn_s_sleep(1);
    }
    __builtin_amdgcn_fence(__ATOMIC_ACQUIRE, "wavefront");
  }
  unsigned long long r3 = __builtin_amdgcn_s_memrealtime();
  {
    f32x2 ml0 = *(lds_f32x2_p)(uintptr_t)(xml + r32 * 8);
    u32x4 ro[3][2];
    u32x2 rml[3];
#pragma unroll
    for (int i = 0; i < 3; ++i) {
      const int sg = (g + 1 + i) & 3;
      const int slab = (unit * 4 + g) * 4 + sg;
      rml[i] = __builtin_amdgcn_raw_buffer_load_b64(rsrc, (unsigned)(WS_ML + slab * 256) + r32 * 8, 0, 16);
#pragma unroll
      for (int hh = 0; hh < 2; ++hh)
        ro[i][hh] = __builtin_amdgcn_raw_buffer_load_b128(rsrc, (unsigned)(WS_O + (size_t)slab * 8192) + lane * 16 + (wid * 2 + hh) * 1024, 0, 16);
    }
    f32x4 own[4];
#pragma unroll
    for (int gq = 0; gq < 4; ++gq) own[gq] = *(lds_f32x4_p)(uintptr_t)(xo + (wid * 4 + gq) * 1024 + lane * 16);
    float M = ml0[0];
#pragma unroll
    for (int i = 0; i < 3; ++i) M = fmaxf(M, __uint_as_float(rml[i][0]));
    const float f0 = __builtin_amdgcn_exp2f(ml0[0] - M);
    float L = f0 * ml0[1];
    float f[3];
#pragma unroll
    for (int i = 0; i < 3; ++i) {
      f[i] = __builtin_amdgcn_exp2f(__uint_as_float(rml[i][0]) - M);
      L += f[i] * __uint_as_float(rml[i][1]);
    }
    const float inv = 1.f / L;
    char* orow = (char*)Op + ((size_t)(unit * 4 + g) * SQ + r32) * D * 2 + wid * 64 + h * 8;
#pragma unroll
    for (int gq = 0; gq < 4; ++gq) {
      float a[4];
#pragma unroll
      for (int e = 0; e < 4; ++e) {
        float x = f0 * own[gq][e];
#pragma unroll
        for (int i = 0; i < 3; ++i) {
          const unsigned wd = ro[i][gq >> 1][(gq & 1) * 2 + (e >> 1)];
          x += f[i] * __uint_as_float((e & 1) ? (wd & 0xffff0000u) : (wd << 16));
        }
        a[e] = x * inv;
      }
      u32x2 w = {pk_bf16(a[0], a[1]), pk_bf16(a[2], a[3])};
      *(u32x2*)(orow + gq * 16) = w;
    }
  }
  {
    asm volatile("s_waitcnt vmcnt(0)" ::: "memory");
    unsigned long long r4 = __builtin_amdgcn_s_memrealtime();
    if (bid == PROBE_BID && wid == 0) {
      unsigned long long d = PROBE == 1 ? 10 * (r1 - rt0) : PROBE == 2 ? 10 * (r2 - r1) : PROBE == 3 ? 10 * (r3 - r2) : PROBE == 4 ? 10 * (r4 - r3) : PROBE == 5 ? 10 * (w1 - w0) : 0;
      while (__builtin_amdgcn_s_memrealtime() < r4 + d) __builtin_amdgcn_s_sleep(2);
    }
  }
  __syncthreads();
  if (tid == 128) *(unsigned long long*)(ws + WS_E + (unit * 4 + g) * 8) = tag_own;
}

extern "C" void kernel_launch(void* const* d_in, const int* in_sizes, int n_in, void* d_out, int out_size, void* d_ws, size_t ws_size,
                              hipStream_t stream) {
  (void)in_sizes; (void)n_in; (void)out_size; (void)ws_size;
  (void)hipFuncSetAttribute((const void*)gqa_kernel, hipFuncAttributeMaxDynamicSharedMemorySize, LDS_BYTES);
  hipLaunchKernelGGL(gqa_kernel, dim3(256), dim3(384), LDS_BYTES, stream, (const __hip_bfloat16*)d_in[0], (const __hip_bfloat16*)d_in[1],
                     (const __hip_bfloat16*)d_in[2], (__hip_bfloat16*)d_out, (char*)d_ws);
}
```

```cpp
#define PROBE 0
#define PROBE_BID 100
#include <hip/hip_runtime.h>
#include <hip/hip_bf16.h>

typedef short bf16x8 __attribute__((ext_vector_type(8)));
typedef short s16x4 __attribute__((ext_vector_type(4)));
typedef float f32x16 __attribute__((ext_vector_type(16)));
typedef float f32x4 __attribute__((ext_vector_type(4)));
typedef float f32x2 __attribute__((ext_vector_type(2)));
typedef __bf16 bf16x2_t __attribute__((ext_vector_type(2)));
typedef unsigned u32x4 __attribute__((ext_vector_type(4)));
typedef unsigned u32x2 __attribute__((ext_vector_type(2)));
typedef __attribute__((address_space(3))) s16x4* lds_s16x4_p;
typedef __attribute__((address_space(3))) bf16x8* lds_bf16x8_p;
typedef __attribute__((address_space(3))) f32x4* lds_f32x4_p;
typedef __attribute__((address_space(3))) f32x2* lds_f32x2_p;
typedef __attribute__((address_space(1))) unsigned long long gu64;

constexpr int SQ = 32, SK = 2048, D = 128;
constexpr int NSPLIT = 4, KT = 32, KPW = SK / NSPLIT, NT = KPW / KT;
constexpr int NS = 8;
#define LA 4
static_assert(LA <= NS - 3, "lookahead");
constexpr int TILE_B = KT * D * 2;
constexpr int LDS_BYTES = NS * 2 * TILE_B;
constexpr float SC = 0.088388347648318447f * 1.4426950408889634f;
constexpr float THR = 8.f;
constexpr size_t WS_E = 0, WS_F = 4096, WS_ML = 65536, WS_O = 1 << 20;
constexpr size_t WS_END = WS_O + (size_t)64 * 4 * 4 * 16384;

__device__ __forceinline__ unsigned pk_bf16(float a, float b) {
  f32x2 v = {a, b};
  bf16x2_t r = __builtin_convertvector(v, bf16x2_t);
  return __builtin_bit_cast(unsigned, r);
}

__device__ __forceinline__ void dma8(const char* gbase, unsigned lds, unsigned o0, unsigned o1, unsigned o2, unsigned o3) {
  unsigned keep;
  const char* g2 = gbase + 4096;
  const unsigned lds2 = lds + 4096;
  asm volatile(
      "s_mov_b32 %0, m0\n\t"
      "s_mov_b32 m0, %7\n\t"
      "s_nop 0\n\t"
      "global_load_lds_dwordx4 %1, %5 offset:0 nt\n\t"
      "global_load_lds_dwordx4 %2, %5 offset:1024 nt\n\t"
      "global_load_lds_dwordx4 %3, %5 offset:2048 nt\n\t"
      "global_load_lds_dwordx4 %4, %5 offset:3072 nt\n\t"
      "s_mov_b32 m0, %8\n\t"
      "s_nop 0\n\t"
      "global_load_lds_dwordx4 %1, %6 offset:0 nt\n\t"
      "global_load_lds_dwordx4 %2, %6 offset:1024 nt\n\t"
      "global_load_lds_dwordx4 %3, %6 offset:2048 nt\n\t"
      "global_load_lds_dwordx4 %4, %6 offset:3072 nt\n\t"
      "s_mov_b32 m0, %0"
      : "=&s"(keep)
      : "v"(o0), "v"(o1), "v"(o2), "v"(o3), "s"(gbase), "s"(g2), "s"(lds), "s"(lds2)
      : "memory");
}

template <int N> __device__ __forceinline__ void wait_bar() { asm volatile("s_waitcnt vmcnt(%0)\n\ts_barrier" ::"i"(N) : "memory"); }
#define RAMP 2
template <int T, int ISS> __device__ __forceinline__ void loader_steps(const char* src, unsigned dst0, unsigned o0, unsigned o1, unsigned o2, unsigned o3) {
  if constexpr (T < NT) {
    wait_bar<8 * (ISS - T - 1)>();
    constexpr int want = T + 1 + LA < NT ? T + 1 + LA : NT;
    constexpr int nxt = ISS + RAMP < want ? ISS + RAMP : (want > ISS ? want : ISS);
#pragma unroll
    for (int t = ISS; t < nxt; ++t) dma8(src + (size_t)t * TILE_B, dst0 + (t % NS) * 2 * TILE_B, o0, o1, o2, o3);
    loader_steps<T + 1, nxt>(src, dst0, o0, o1, o2, o3);
  }
}
template <int I> __device__ __forceinline__ void tail_waits() {
  if constexpr (I >= 0) { wait_bar<8 * I>(); tail_waits<I - 1>(); }
}

__global__ __launch_bounds__(384, 2) void gqa_kernel(const __hip_bfloat16* __restrict__ Qp, const __hip_bfloat16* __restrict__ Kp,
                                                      const __hip_bfloat16* __restrict__ Vp, __hip_bfloat16* __restrict__ Op, char* __restrict__ ws) {
  extern __shared__ __attribute__((aligned(16))) char smem[];
  const int tid = threadIdx.x, lane = tid & 63;
  const int wv = __builtin_amdgcn_readfirstlane(tid >> 6);
  const int bid = blockIdx.x, xcd = bid & 7, idx = bid >> 3;
  const int unit = xcd * 8 + (idx >> 2), g = idx & 3;
  const unsigned smem_base = (unsigned)(uintptr_t)smem;
  unsigned long long rt0 = __builtin_amdgcn_s_memrealtime();

  if (__builtin_expect(wv < 2, 1)) {
    const int li = lane & 15, lq = lane >> 4;
    const unsigned sw = wv == 0 ? lq : (lq << 2);
    const unsigned o0 = lq * 256 + ((li ^ sw) * 16);
    const unsigned x = wv == 0 ? 64 : 0;
    const unsigned o1 = o0 ^ x, o2 = o0 ^ (2 * x), o3 = o0 ^ (3 * x);
    const char* src = (wv == 0 ? (const char*)Kp : (const char*)Vp) + ((size_t)unit * SK + (size_t)g * KPW) * D * 2;
    const unsigned dst0 = smem_base + wv * TILE_B;
#define P0 LA
#pragma unroll
    for (int t = 0; t < P0; ++t) dma8(src + (size_t)t * TILE_B, dst0 + t * 2 * TILE_B, o0, o1, o2, o3);
    loader_steps<0, P0>(src, dst0, o0, o1, o2, o3);
    asm volatile("s_barrier" ::: "memory");
    return;
  }
  const int wid = wv - 2;
  const int r32 = lane & 31, h = lane >> 5;
  bf16x8 qf[8];
  {
    const char* Qb = (const char*)Qp + (size_t)(unit * 4 + wid) * SQ * D * 2 + r32 * 256 + h * 16;
#pragma unroll
    for (int ks = 0; ks < 8; ++ks) {
      const u32x4 raw = *(const u32x4*)(Qb + ks * 32);
      u32x4 w;
#pragma unroll
      for (int e = 0; e < 4; ++e)
        w[e] = pk_bf16(__uint_as_float(raw[e] << 16) * SC, __uint_as_float(raw[e] & 0xffff0000u) * SC);
      qf[ks] = __builtin_bit_cast(bf16x8, w);
    }
  }
  const unsigned long long tag_pub = *(const unsigned long long*)(ws + WS_E + (unit * 4 + wid) * 8) + 1;
  const unsigned long long tag_own = *(const unsigned long long*)(ws + WS_E + (unit * 4 + g) * 8) + 1;

  const unsigned kr = smem_base + r32 * 256 + ((h ^ (r32 & 15)) * 16);
  const int tq = (lane & 15) >> 2, tp = lane & 3, tblk = (lane >> 4) & 1;
  const unsigned vr = smem_base + TILE_B + 256 * (4 * h + tq) + 32 * tblk + 8 * tp;

  f32x16 o[4];
#pragma unroll
  for (int i = 0; i < 4; ++i)
#pragma unroll
    for (int j = 0; j < 16; ++j) o[i][j] = 0.f;
  float m, l = 0.f;
  f32x16 sa, sb, negm;
  bf16x8 kf[8];
  s16x4 vlo[8], vhi[8];
  unsigned ka[8], va[4];
#pragma unroll
  for (int ks = 0; ks < 8; ++ks) ka[ks] = kr ^ (32 * ks);
#pragma unroll
  for (int db = 0; db < 4; ++db) va[db] = vr + 64 * (db ^ tq);
#define SB() __builtin_amdgcn_sched_barrier(0)
#define PIN(v) asm volatile("" : "+v"(v))
#define KREAD(ks, tile) kf[ks] = *(lds_bf16x8_p)(uintptr_t)(ka[ks] + ((tile) & 3) * 2 * TILE_B)
#define VREAD(i, tile)                                                                                                                         \
  do {                                                                                                                                         \
    vlo[i] = __builtin_amdgcn_ds_read_tr16_b64_v4i16((lds_s16x4_p)(uintptr_t)(va[(i) & 3] + 4096 * ((i) >> 2) + ((tile) & 3) * 2 * TILE_B));        \
    vhi[i] = __builtin_amdgcn_ds_read_tr16_b64_v4i16((lds_s16x4_p)(uintptr_t)(va[(i) & 3] + 4096 * ((i) >> 2) + 2048 + ((tile) & 3) * 2 * TILE_B)); \
  } while (0)
  {
    const f32x16 zero = {};
    asm volatile("s_barrier" ::: "memory");
#pragma unroll
    for (int ks = 0; ks < 8; ++ks) KREAD(ks, 0);
    sa = __builtin_amdgcn_mfma_f32_32x32x16_bf16(kf[0], qf[0], zero, 0, 0, 0);
#pragma unroll
    for (int ks = 1; ks < 8; ++ks) sa = __builtin_amdgcn_mfma_f32_32x32x16_bf16(kf[ks], qf[ks], sa, 0, 0, 0);
    SB();
    asm volatile("s_barrier" ::: "memory");
#pragma unroll
    for (int ks = 0; ks < 8; ++ks) KREAD(ks, 1);
    float mx = sa[0];
#pragma unroll
    for (int j = 1; j < 16; ++j) mx = fmaxf(mx, sa[j]);
    auto rr = __builtin_amdgcn_permlane32_swap(__float_as_uint(mx), __float_as_uint(mx), false, false);
    m = fmaxf(__uint_as_float(rr[0]), __uint_as_float(rr[1]));
#pragma unroll
    for (int j = 0; j < 16; ++j) { sa[j] -= m; negm[j] = -m; }
    SB();
  }
#define ITER(t, S, X)                                                                                              \
  {                                                                                                                \
    unsigned pw[8];                                                                                                \
    _Pragma("unroll") for (int k = 0; k < 8; ++k) {                                                                \
      if (k == 0) S = __builtin_amdgcn_mfma_f32_32x32x16_bf16(kf[0], qf[0], negm, 0, 0, 0);                        \
      else S = __builtin_amdgcn_mfma_f32_32x32x16_bf16(kf[k], qf[k], S, 0, 0, 0);                                  \
      SB();                                                                                                        \
      VREAD(k, (t) - 1);                                                                                           \
      X[2 * k] = __builtin_amdgcn_exp2f(X[2 * k]);                                                                 \
      X[2 * k + 1] = __builtin_amdgcn_exp2f(X[2 * k + 1]);                                                         \
      if (k > 0) {                                                                                                 \
        pw[k - 1] = pk_bf16(X[2 * k - 2], X[2 * k - 1]);                                                           \
        if (k & 1) { l += X[2 * k - 2]; PIN(l); }                                                                  \
      }                                                                                                            \
      SB();                                                                                                        \
    }                                                                                                              \
    pw[7] = pk_bf16(X[14], X[15]);                                                                                 \
    asm volatile("s_barrier" ::: "memory");                     \
    const bf16x8 pf0 = __builtin_bit_cast(bf16x8, u32x4{pw[0], pw[1], pw[2], pw[3]});                              \
    const bf16x8 pf1 = __builtin_bit_cast(bf16x8, u32x4{pw[4], pw[5], pw[6], pw[7]});                              \
    float mx = 0.f;                                                                                                \
    SB();                                                                                                          \
    _Pragma("unroll") for (int k = 0; k < 8; ++k) {                                                                \
      const bf16x8 vf = __builtin_shufflevector(vlo[k], vhi[k], 0, 1, 2, 3, 4, 5, 6, 7);                           \
      o[k & 3] = __builtin_amdgcn_mfma_f32_32x32x16_bf16(vf, k < 4 ? pf0 : pf1, o[k & 3], 0, 0, 0);                \
      SB();                                                                                                        \
      if ((((t) + 1) & 3) == 0) { ka[k] += ((((t) + 1) & 4) ? 8 * TILE_B : -8 * TILE_B); PIN(ka[k]); }                                               \
      if ((t) + 1 < NT) KREAD(k, (t) + 1);                                                                         \
      if ((((t)) & 3) == 0 && k < 4) { va[k] += (((t) & 4) ? 8 * TILE_B : -8 * TILE_B); PIN(va[k]); }                                          \
                                             \
      l += X[2 * k + 1];                                                                                           \
      if (k & 1) l += X[2 * k];                                                                                    \
      PIN(l);                                                                                                      \
      if (k == 2) { PIN(S); mx = fmaxf(fmaxf(S[0], S[1]), S[2]); mx = fmaxf(fmaxf(mx, S[3]), S[4]); PIN(mx); }     \
      if (k == 3) { mx = fmaxf(fmaxf(mx, S[5]), S[6]); mx = fmaxf(fmaxf(mx, S[7]), S[8]); PIN(mx); }               \
      if (k == 4) { mx = fmaxf(fmaxf(mx, S[9]), S[10]); mx = fmaxf(fmaxf(mx, S[11]), S[12]); PIN(mx); }            \
      if (k == 5) { mx = fmaxf(fmaxf(mx, S[13]), S[14]); mx = fmaxf(mx, S[15]); PIN(mx); }                         \
      if (k == 6) {                                                                                                \
        auto rr = __builtin_amdgcn_permlane32_swap(__float_as_uint(mx), __float_as_uint(mx), false, false);        \
        mx = fmaxf(__uint_as_float(rr[0]), __uint_as_float(rr[1]));                                                \
      }                                                                                                            \
      SB();                                                                                                        \
    }                                                                                                              \
    if (RESCALE_COND(mx)) {   \
      const float d = fmaxf(mx, 0.f);                                                                              \
      const float alpha = __builtin_amdgcn_exp2f(-d);                                                              \
      m += d;                                                                                                      \
      l *= alpha;                                                                                                  \
      _Pragma("unroll") for (int j = 0; j < 16; ++j) { S[j] -= d; negm[j] = -m; }                                  \
      _Pragma("unroll") for (int i = 0; i < 4; ++i) _Pragma("unroll") for (int j = 0; j < 16; ++j) o[i][j] *= alpha; \
    }                                                                                                              \
    SB();                                                                                                          \
  }
#define RESCALE_COND(mx) (__any(!(mx <= THR)))
  ITER(1, sb, sa) ITER(2, sa, sb) ITER(3, sb, sa) ITER(4, sa, sb) ITER(5, sb, sa) ITER(6, sa, sb) ITER(7, sb, sa) ITER(8, sa, sb)
  ITER(9, sb, sa) ITER(10, sa, sb) ITER(11, sb, sa) ITER(12, sa, sb) ITER(13, sb, sa) ITER(14, sa, sb) ITER(15, sb, sa)
  {
#pragma unroll
    for (int k = 0; k < 8; ++k) VREAD(k, NT - 1);
#pragma unroll
    for (int j = 0; j < 16; ++j) sb[j] = __builtin_amdgcn_exp2f(sb[j]);
    const bf16x8 pf0 = __builtin_bit_cast(bf16x8, u32x4{pk_bf16(sb[0], sb[1]), pk_bf16(sb[2], sb[3]), pk_bf16(sb[4], sb[5]), pk_bf16(sb[6], sb[7])});
    const bf16x8 pf1 = __builtin_bit_cast(bf16x8, u32x4{pk_bf16(sb[8], sb[9]), pk_bf16(sb[10], sb[11]), pk_bf16(sb[12], sb[13]), pk_bf16(sb[14], sb[15])});
#pragma unroll
    for (int k = 0; k < 8; ++k) {
      const bf16x8 vf = __builtin_shufflevector(vlo[k], vhi[k], 0, 1, 2, 3, 4, 5, 6, 7);
      o[k & 3] = __builtin_amdgcn_mfma_f32_32x32x16_bf16(vf, k < 4 ? pf0 : pf1, o[k & 3], 0, 0, 0);
    }
#pragma unroll
    for (int j = 0; j < 16; ++j) l += sb[j];
  }

  asm volatile("" :: "v"(o[0][0]), "v"(o[1][0]), "v"(o[2][0]), "v"(o[3][0]));
  unsigned long long r1 = __builtin_amdgcn_s_memrealtime();
  {
    auto rr = __builtin_amdgcn_permlane32_swap(__float_as_uint(l), __float_as_uint(l), false, false);
    l = __uint_as_float(rr[0]) + __uint_as_float(rr[1]);
  }
  const auto rsrc = __builtin_amdgcn_make_buffer_rsrc((void*)ws, 0, (int)WS_END, 0x00020000);
  const unsigned xo = smem_base;
  const unsigned xml = smem_base + 16384;
  if (wid != g) {
    const int slab = (unit * 4 + wid) * 4 + g;
    const unsigned ob = (unsigned)(WS_O + (size_t)slab * 8192) + lane * 16;
#pragma unroll
    for (int db = 0; db < 4; ++db)
#pragma unroll
      for (int hh = 0; hh < 2; ++hh) {
        u32x4 v = {pk_bf16(o[db][8 * hh], o[db][8 * hh + 1]), pk_bf16(o[db][8 * hh + 2], o[db][8 * hh + 3]), pk_bf16(o[db][8 * hh + 4], o[db][8 * hh + 5]),
                   pk_bf16(o[db][8 * hh + 6], o[db][8 * hh + 7])};
        __builtin_amdgcn_raw_buffer_store_b128(v, rsrc, ob + (db * 2 + hh) * 1024, 0, 16);
      }
    if (h == 0) {
      u32x2 v = {__float_as_uint(m), __float_as_uint(l)};
      __builtin_amdgcn_raw_buffer_store_b64(v, rsrc, (unsigned)(WS_ML + slab * 256) + r32 * 8, 0, 16);
    }
    asm volatile("s_waitcnt vmcnt(0)" ::: "memory");
    if (lane == 0) __hip_atomic_store((gu64*)(ws + WS_F + slab * 8), tag_pub, __ATOMIC_RELAXED, __HIP_MEMORY_SCOPE_AGENT);
  } else {
#pragma unroll
    for (int db = 0; db < 4; ++db)
#pragma unroll
      for (int gq = 0; gq < 4; ++gq) {
        f32x4 v = {o[db][4 * gq], o[db][4 * gq + 1], o[db][4 * gq + 2], o[db][4 * gq + 3]};
        *(lds_f32x4_p)(uintptr_t)(xo + (db * 4 + gq) * 1024 + lane * 16) = v;
      }
    if (h == 0) {
      f32x2 v = {m, l};
      *(lds_f32x2_p)(uintptr_t)(xml + r32 * 8) = v;
    }
  }
  __syncthreads();
  unsigned long long r2 = __builtin_amdgcn_s_memrealtime();
  {
    const gu64* fp = (const gu64*)(ws + WS_F + (size_t)((unit * 4 + g) * 4) * 8);
    const int sl = lane & 3;
#pragma unroll 1
    for (unsigned spins = 0; spins < 20000000u; ++spins) {
      const unsigned long long v = __hip_atomic_load(fp + sl, __ATOMIC_RELAXED, __HIP_MEMORY_SCOPE_AGENT);
      const bool ok = (sl == g) || (v == tag_own);
      if (__all(ok)) break;
      __builtin_amdgcn_s_sleep(1);
    }
    __builtin_amdgcn_fence(__ATOMIC_ACQUIRE, "wavefront");
  }
  unsigned long long r3 = __builtin_amdgcn_s_memrealtime();
  {
    f32x2 ml0 = *(lds_f32x2_p)(uintptr_t)(xml + r32 * 8);
    u32x4 ro[3][2];
    u32x2 rml[3];
#pragma unroll
    for (int i = 0; i < 3; ++i) {
      const int sg = (g + 1 + i) & 3;
      const int slab = (unit * 4 + g) * 4 + sg;
      rml[i] = __builtin_amdgcn_raw_buffer_load_b64(rsrc, (unsigned)(WS_ML + slab * 256) + r32 * 8, 0, 16);
#pragma unroll
      for (int hh = 0; hh < 2; ++hh)
        ro[i][hh] = __builtin_amdgcn_raw_buffer_load_b128(rsrc, (unsigned)(WS_O + (size_t)slab * 8192) + lane * 16 + (wid * 2 + hh) * 1024, 0, 16);
    }
    f32x4 own[4];
#pragma unroll
    for (int gq = 0; gq < 4; ++gq) own[gq] = *(lds_f32x4_p)(uintptr_t)(xo + (wid * 4 + gq) * 1024 + lane * 16);
    float M = ml0[0];
#pragma unroll
    for (int i = 0; i < 3; ++i) M = fmaxf(M, __uint_as_float(rml[i][0]));
    const float f0 = __builtin_amdgcn_exp2f(ml0[0] - M);
    float L = f0 * ml0[1];
    float f[3];
#pragma unroll
    for (int i = 0; i < 3; ++i) {
      f[i] = __builtin_amdgcn_exp2f(__uint_as_float(rml[i][0]) - M);
      L += f[i] * __uint_as_float(rml[i][1]);
    }
    const float inv = 1.f / L;
    char* orow = (char*)Op + ((size_t)(unit * 4 + g) * SQ + r32) * D * 2 + wid * 64 + h * 8;
#pragma unroll
    for (int gq = 0; gq < 4; ++gq) {
      float a[4];
#pragma unroll
      for (int e = 0; e < 4; ++e) {
        float x = f0 * own[gq][e];
#pragma unroll
        for (int i = 0; i < 3; ++i) {
          const unsigned wd = ro[i][gq >> 1][(gq & 1) * 2 + (e >> 1)];
          x += f[i] * __uint_as_float((e & 1) ? (wd & 0xffff0000u) : (wd << 16));
        }
        a[e] = x * inv;
      }
      u32x2 w = {pk_bf16(a[0], a[1]), pk_bf16(a[2], a[3])};
      *(u32x2*)(orow + gq * 16) = w;
    }
  }
  {
    asm volatile("s_waitcnt vmcnt(0)" ::: "memory");
    unsigned long long r4 = __builtin_amdgcn_s_memrealtime();
    if ((PROBE_BID < 0 || bid == PROBE_BID) && wid == 0) {
      unsigned long long d = PROBE == 1 ? 10 * (r1 - rt0) : PROBE == 2 ? 10 * (r2 - r1) : PROBE == 3 ? 10 * (r3 - r2) : PROBE == 4 ? 10 * (r4 - r3) : PROBE == 5 ? 10 * (r4 - rt0) : 0;
      while (__builtin_amdgcn_s_memrealtime() < r4 + d) __builtin_amdgcn_s_sleep(2);
    }
  }
  __syncthreads();
  if (tid == 128) *(unsigned long long*)(ws + WS_E + (unit * 4 + g) * 8) = tag_own;
}

extern "C" void kernel_launch(void* const* d_in, const int* in_sizes, int n_in, void* d_out, int out_size, void* d_ws, size_t ws_size,
                              hipStream_t stream) {
  (void)in_sizes; (void)n_in; (void)out_size; (void)ws_size;
  (void)hipFuncSetAttribute((const void*)gqa_kernel, hipFuncAttributeMaxDynamicSharedMemorySize, LDS_BYTES);
  hipLaunchKernelGGL(gqa_kernel, dim3(256), dim3(384), LDS_BYTES, stream, (const __hip_bfloat16*)d_in[0], (const __hip_bfloat16*)d_in[1],
                     (const __hip_bfloat16*)d_in[2], (__hip_bfloat16*)d_out, (char*)d_ws);
}
```

```cpp
#include <hip/hip_runtime.h>
#include <hip/hip_bf16.h>

typedef short bf16x8 __attribute__((ext_vector_type(8)));
typedef short s16x4 __attribute__((ext_vector_type(4)));
typedef float f32x16 __attribute__((ext_vector_type(16)));
typedef float f32x4 __attribute__((ext_vector_type(4)));
typedef float f32x2 __attribute__((ext_vector_type(2)));
typedef __bf16 bf16x2_t __attribute__((ext_vector_type(2)));
typedef unsigned u32x4 __attribute__((ext_vector_type(4)));
typedef unsigned u32x2 __attribute__((ext_vector_type(2)));
typedef __attribute__((address_space(3))) s16x4* lds_s16x4_p;
typedef __attribute__((address_space(3))) bf16x8* lds_bf16x8_p;
typedef __attribute__((address_space(3))) f32x4* lds_f32x4_p;
typedef __attribute__((address_space(3))) f32x2* lds_f32x2_p;
typedef __attribute__((address_space(1))) unsigned long long gu64;

constexpr int SQ = 32, SK = 2048, D = 128;
constexpr int NSPLIT = 4, KT = 32, KPW = SK / NSPLIT, NT = KPW / KT;
constexpr int NS = 8;
#define LA 4
static_assert(LA <= NS - 3, "lookahead");
constexpr int TILE_B = KT * D * 2;
constexpr int LDS_BYTES = NS * 2 * TILE_B;
constexpr float SC = 0.088388347648318447f * 1.4426950408889634f;
constexpr float THR = 8.f;
constexpr size_t WS_E = 0, WS_F = 4096, WS_ML = 65536, WS_O = 1 << 20;
constexpr size_t WS_END = WS_O + (size_t)64 * 4 * 4 * 16384;

__device__ __forceinline__ unsigned pk_bf16(float a, float b) {
  f32x2 v = {a, b};
  bf16x2_t r = __builtin_convertvector(v, bf16x2_t);
  return __builtin_bit_cast(unsigned, r);
}

__device__ __forceinline__ void dma8(const char* gbase, unsigned lds, unsigned o0, unsigned o1, unsigned o2, unsigned o3) {
  unsigned keep;
  const char* g2 = gbase + 4096;
  const unsigned lds2 = lds + 4096;
  asm volatile(
      "s_mov_b32 %0, m0\n\t"
      "s_mov_b32 m0, %7\n\t"
      "s_nop 0\n\t"
      "global_load_lds_dwordx4 %1, %5 offset:0\n\t"
      "global_load_lds_dwordx4 %2, %5 offset:1024\n\t"
      "global_load_lds_dwordx4 %3, %5 offset:2048\n\t"
      "global_load_lds_dwordx4 %4, %5 offset:3072\n\t"
      "s_mov_b32 m0, %8\n\t"
      "s_nop 0\n\t"
      "global_load_lds_dwordx4 %1, %6 offset:0\n\t"
      "global_load_lds_dwordx4 %2, %6 offset:1024\n\t"
      "global_load_lds_dwordx4 %3, %6 offset:2048\n\t"
      "global_load_lds_dwordx4 %4, %6 offset:3072\n\t"
      "s_mov_b32 m0, %0"
      : "=&s"(keep)
      : "v"(o0), "v"(o1), "v"(o2), "v"(o3), "s"(gbase), "s"(g2), "s"(lds), "s"(lds2)
      : "memory");
}

template <int N> __device__ __forceinline__ void wait_bar() { asm volatile("s_waitcnt vmcnt(%0)\n\ts_barrier" ::"i"(N) : "memory"); }
#define RAMP 2
template <int T, int ISS> __device__ __forceinline__ void loader_steps(const char* src, unsigned dst0, unsigned o0, unsigned o1, unsigned o2, unsigned o3) {
  if constexpr (T < NT) {
    wait_bar<8 * (ISS - T - 1)>();
    constexpr int want = T + 1 + LA < NT ? T + 1 + LA : NT;
    constexpr int nxt = ISS + RAMP < want ? ISS + RAMP : (want > ISS ? want : ISS);
#pragma unroll
    for (int t = ISS; t < nxt; ++t) dma8(src + (size_t)t * TILE_B, dst0 + (t % NS) * 2 * TILE_B, o0, o1, o2, o3);
    loader_steps<T + 1, nxt>(src, dst0, o0, o1, o2, o3);
  }
}
template <int I> __device__ __forceinline__ void tail_waits() {
  if constexpr (I >= 0) { wait_bar<8 * I>(); tail_waits<I - 1>(); }
}

__global__ __launch_bounds__(384, 2) void gqa_kernel(const __hip_bfloat16* __restrict__ Qp, const __hip_bfloat16* __restrict__ Kp,
                                                      const __hip_bfloat16* __restrict__ Vp, __hip_bfloat16* __restrict__ Op, char* __restrict__ ws) {
  extern __shared__ __attribute__((aligned(16))) char smem[];
  const int tid = threadIdx.x, lane = tid & 63;
  const int wv = __builtin_amdgcn_readfirstlane(tid >> 6);
  const int bid = blockIdx.x, xcd = bid & 7, idx = bid >> 3;
  const int unit = xcd * 8 + (idx >> 2), g = idx & 3;
  const unsigned smem_base = (unsigned)(uintptr_t)smem;

  if (__builtin_expect(wv < 2, 1)) {
    const int li = lane & 15, lq = lane >> 4;
    const unsigned sw = wv == 0 ? lq : (lq << 2);
    const unsigned o0 = lq * 256 + ((li ^ sw) * 16);
    const unsigned x = wv == 0 ? 64 : 0;
    const unsigned o1 = o0 ^ x, o2 = o0 ^ (2 * x), o3 = o0 ^ (3 * x);
    const char* src = (wv == 0 ? (const char*)Kp : (const char*)Vp) + ((size_t)unit * SK + (size_t)g * KPW) * D * 2;
    const unsigned dst0 = smem_base + wv * TILE_B;
#define P0 LA
#pragma unroll
    for (int t = 0; t < P0; ++t) dma8(src + (size_t)t * TILE_B, dst0 + t * 2 * TILE_B, o0, o1, o2, o3);
    loader_steps<0, P0>(src, dst0, o0, o1, o2, o3);
    asm volatile("s_barrier" ::: "memory");
    return;
  }
  const int wid = wv - 2;
  const int r32 = lane & 31, h = lane >> 5;
  bf16x8 qf[8];
  {
    const char* Qb = (const char*)Qp + (size_t)(unit * 4 + wid) * SQ * D * 2 + r32 * 256 + h * 16;
#pragma unroll
    for (int ks = 0; ks < 8; ++ks) {
      const u32x4 raw = *(const u32x4*)(Qb + ks * 32);
      u32x4 w;
#pragma unroll
      for (int e = 0; e < 4; ++e)
        w[e] = pk_bf16(__uint_as_float(raw[e] << 16) * SC, __uint_as_float(raw[e] & 0xffff0000u) * SC);
      qf[ks] = __builtin_bit_cast(bf16x8, w);
    }
  }
  const unsigned long long tag_pub = *(const unsigned long long*)(ws + WS_E + (unit * 4 + wid) * 8) + 1;
  const unsigned long long tag_own = *(const unsigned long long*)(ws + WS_E + (unit * 4 + g) * 8) + 1;

  const unsigned kr = smem_base + r32 * 256 + ((h ^ (r32 & 15)) * 16);
  const int tq = (lane & 15) >> 2, tp = lane & 3, tblk = (lane >> 4) & 1;
  const unsigned vr = smem_base + TILE_B + 256 * (4 * h + tq) + 32 * tblk + 8 * tp;

  f32x16 o[4];
#pragma unroll
  for (int i = 0; i < 4; ++i)
#pragma unroll
    for (int j = 0; j < 16; ++j) o[i][j] = 0.f;
  float m, l = 0.f;
  f32x16 sa, sb, negm;
  bf16x8 kf[8];
  s16x4 vlo[8], vhi[8];
  unsigned ka[8], va[4];
#pragma unroll
  for (int ks = 0; ks < 8; ++ks) ka[ks] = kr ^ (32 * ks);
#pragma unroll
  for (int db = 0; db < 4; ++db) va[db] = vr + 64 * (db ^ tq);
#define SB() __builtin_amdgcn_sched_barrier(0)
#define PIN(v) asm volatile("" : "+v"(v))
#define KREAD(ks, tile) kf[ks] = *(lds_bf16x8_p)(uintptr_t)(ka[ks] + ((tile) & 3) * 2 * TILE_B)
#define VREAD(i, tile)                                                                                                                         \
  do {                                                                                                                                         \
    vlo[i] = __builtin_amdgcn_ds_read_tr16_b64_v4i16((lds_s16x4_p)(uintptr_t)(va[(i) & 3] + 4096 * ((i) >> 2) + ((tile) & 3) * 2 * TILE_B));        \
    vhi[i] = __builtin_amdgcn_ds_read_tr16_b64_v4i16((lds_s16x4_p)(uintptr_t)(va[(i) & 3] + 4096 * ((i) >> 2) + 2048 + ((tile) & 3) * 2 * TILE_B)); \
  } while (0)
  {
    const f32x16 zero = {};
    asm volatile("s_barrier" ::: "memory");
#pragma unroll
    for (int ks = 0; ks < 8; ++ks) KREAD(ks, 0);
    sa = __builtin_amdgcn_mfma_f32_32x32x16_bf16(kf[0], qf[0], zero, 0, 0, 0);
#pragma unroll
    for (int ks = 1; ks < 8; ++ks) sa = __builtin_amdgcn_mfma_f32_32x32x16_bf16(kf[ks], qf[ks], sa, 0, 0, 0);
    SB();
    asm volatile("s_barrier" ::: "memory");
#pragma unroll
    for (int ks = 0; ks < 8; ++ks) KREAD(ks, 1);
    float mx = sa[0];
#pragma unroll
    for (int j = 1; j < 16; ++j) mx = fmaxf(mx, sa[j]);
    auto rr = __builtin_amdgcn_permlane32_swap(__float_as_uint(mx), __float_as_uint(mx), false, false);
    m = fmaxf(__uint_as_float(rr[0]), __uint_as_float(rr[1]));
#pragma unroll
    for (int j = 0; j < 16; ++j) { sa[j] -= m; negm[j] = -m; }
    SB();
  }
#define ITER(t, S, X)                                                                                              \
  {                                                                                                                \
    unsigned pw[8];                                                                                                \
    _Pragma("unroll") for (int k = 0; k < 8; ++k) {                                                                \
      if (k == 0) S = __builtin_amdgcn_mfma_f32_32x32x16_bf16(kf[0], qf[0], negm, 0, 0, 0);                        \
      else S = __builtin_amdgcn_mfma_f32_32x32x16_bf16(kf[k], qf[k], S, 0, 0, 0);                                  \
      SB();                                                                                                        \
      VREAD(k, (t) - 1);                                                                                           \
      X[2 * k] = __builtin_amdgcn_exp2f(X[2 * k]);                                                                 \
      X[2 * k + 1] = __builtin_amdgcn_exp2f(X[2 * k + 1]);                                                         \
      if (k > 0) {                                                                                                 \
        pw[k - 1] = pk_bf16(X[2 * k - 2], X[2 * k - 1]);                                                           \
        if (k & 1) { l += X[2 * k - 2]; PIN(l); }                                                                  \
      }                                                                                                            \
      SB();                                                                                                        \
    }                                                                                                              \
    pw[7] = pk_bf16(X[14], X[15]);                                                                                 \
    asm volatile("s_barrier" ::: "memory");                     \
    const bf16x8 pf0 = __builtin_bit_cast(bf16x8, u32x4{pw[0], pw[1], pw[2], pw[3]});                              \
    const bf16x8 pf1 = __builtin_bit_cast(bf16x8, u32x4{pw[4], pw[5], pw[6], pw[7]});                              \
    float mx = 0.f;                                                                                                \
    SB();                                                                                                          \
    _Pragma("unroll") for (int k = 0; k < 8; ++k) {                                                                \
      const bf16x8 vf = __builtin_shufflevector(vlo[k], vhi[k], 0, 1, 2, 3, 4, 5, 6, 7);                           \
      o[k & 3] = __builtin_amdgcn_mfma_f32_32x32x16_bf16(vf, k < 4 ? pf0 : pf1, o[k & 3], 0, 0, 0);                \
      SB();                                                                                                        \
      if ((((t) + 1) & 3) == 0) { ka[k] += ((((t) + 1) & 4) ? 8 * TILE_B : -8 * TILE_B); PIN(ka[k]); }                                               \
      if ((t) + 1 < NT) KREAD(k, (t) + 1);                                                                         \
      if ((((t)) & 3) == 0 && k < 4) { va[k] += (((t) & 4) ? 8 * TILE_B : -8 * TILE_B); PIN(va[k]); }                                          \
                                             \
      l += X[2 * k + 1];                                                                                           \
      if (k & 1) l += X[2 * k];                                                                                    \
      PIN(l);                                                                                                      \
      if (k == 2) { PIN(S); mx = fmaxf(fmaxf(S[0], S[1]), S[2]); mx = fmaxf(fmaxf(mx, S[3]), S[4]); PIN(mx); }     \
      if (k == 3) { mx = fmaxf(fmaxf(mx, S[5]), S[6]); mx = fmaxf(fmaxf(mx, S[7]), S[8]); PIN(mx); }               \
      if (k == 4) { mx = fmaxf(fmaxf(mx, S[9]), S[10]); mx = fmaxf(fmaxf(mx, S[11]), S[12]); PIN(mx); }            \
      if (k == 5) { mx = fmaxf(fmaxf(mx, S[13]), S[14]); mx = fmaxf(mx, S[15]); PIN(mx); }                         \
      if (k == 6) {                                                                                                \
        auto rr = __builtin_amdgcn_permlane32_swap(__float_as_uint(mx), __float_as_uint(mx), false, false);        \
        mx = fmaxf(__uint_as_float(rr[0]), __uint_as_float(rr[1]));                                                \
      }                                                                                                            \
      SB();                                                                                                        \
    }                                                                                                              \
    if (RESCALE_COND(mx)) {   \
      const float d = fmaxf(mx, 0.f);                                                                              \
      const float alpha = __builtin_amdgcn_exp2f(-d);                                                              \
      m += d;                                                                                                      \
      l *= alpha;                                                                                                  \
      _Pragma("unroll") for (int j = 0; j < 16; ++j) { S[j] -= d; negm[j] = -m; }                                  \
      _Pragma("unroll") for (int i = 0; i < 4; ++i) _Pragma("unroll") for (int j = 0; j < 16; ++j) o[i][j] *= alpha; \
    }                                                                                                              \
    SB();                                                                                                          \
  }
#define RESCALE_COND(mx) (__any(!(mx <= THR)))
  ITER(1, sb, sa) ITER(2, sa, sb) ITER(3, sb, sa) ITER(4, sa, sb) ITER(5, sb, sa) ITER(6, sa, sb) ITER(7, sb, sa) ITER(8, sa, sb)
  ITER(9, sb, sa) ITER(10, sa, sb) ITER(11, sb, sa) ITER(12, sa, sb) ITER(13, sb, sa) ITER(14, sa, sb) ITER(15, sb, sa)
  {
#pragma unroll
    for (int k = 0; k < 8; ++k) VREAD(k, NT - 1);
#pragma unroll
    for (int j = 0; j < 16; ++j) sb[j] = __builtin_amdgcn_exp2f(sb[j]);
    const bf16x8 pf0 = __builtin_bit_cast(bf16x8, u32x4{pk_bf16(sb[0], sb[1]), pk_bf16(sb[2], sb[3]), pk_bf16(sb[4], sb[5]), pk_bf16(sb[6], sb[7])});
    const bf16x8 pf1 = __builtin_bit_cast(bf16x8, u32x4{pk_bf16(sb[8], sb[9]), pk_bf16(sb[10], sb[11]), pk_bf16(sb[12], sb[13]), pk_bf16(sb[14], sb[15])});
#pragma unroll
    for (int k = 0; k < 8; ++k) {
      const bf16x8 vf = __builtin_shufflevector(vlo[k], vhi[k], 0, 1, 2, 3, 4, 5, 6, 7);
      o[k & 3] = __builtin_amdgcn_mfma_f32_32x32x16_bf16(vf, k < 4 ? pf0 : pf1, o[k & 3], 0, 0, 0);
    }
#pragma unroll
    for (int j = 0; j < 16; ++j) l += sb[j];
  }

  {
    auto rr = __builtin_amdgcn_permlane32_swap(__float_as_uint(l), __float_as_uint(l), false, false);
    l = __uint_as_float(rr[0]) + __uint_as_float(rr[1]);
  }
  const auto rsrc = __builtin_amdgcn_make_buffer_rsrc((void*)ws, 0, (int)WS_END, 0x00020000);
  const unsigned xo = smem_base;
  const unsigned xml = smem_base + 16384;
  if (wid != g) {
    const int slab = (unit * 4 + wid) * 4 + g;
    const unsigned ob = (unsigned)(WS_O + (size_t)slab * 8192) + lane * 16;
#pragma unroll
    for (int db = 0; db < 4; ++db)
#pragma unroll
      for (int hh = 0; hh < 2; ++hh) {
        u32x4 v = {pk_bf16(o[db][8 * hh], o[db][8 * hh + 1]), pk_bf16(o[db][8 * hh + 2], o[db][8 * hh + 3]), pk_bf16(o[db][8 * hh + 4], o[db][8 * hh + 5]),
                   pk_bf16(o[db][8 * hh + 6], o[db][8 * hh + 7])};
        __builtin_amdgcn_raw_buffer_store_b128(v, rsrc, ob + (db * 2 + hh) * 1024, 0, 16);
      }
    if (h == 0) {
      u32x2 v = {__float_as_uint(m), __float_as_uint(l)};
      __builtin_amdgcn_raw_buffer_store_b64(v, rsrc, (unsigned)(WS_ML + slab * 256) + r32 * 8, 0, 16);
    }
    asm volatile("s_waitcnt vmcnt(0)" ::: "memory");
    if (lane == 0) __hip_atomic_store((gu64*)(ws + WS_F + slab * 8), tag_pub, __ATOMIC_RELAXED, __HIP_MEMORY_SCOPE_AGENT);
  } else {
#pragma unroll
    for (int db = 0; db < 4; ++db)
#pragma unroll
      for (int gq = 0; gq < 4; ++gq) {
        f32x4 v = {o[db][4 * gq], o[db][4 * gq + 1], o[db][4 * gq + 2], o[db][4 * gq + 3]};
        *(lds_f32x4_p)(uintptr_t)(xo + (db * 4 + gq) * 1024 + lane * 16) = v;
      }
    if (h == 0) {
      f32x2 v = {m, l};
      *(lds_f32x2_p)(uintptr_t)(xml + r32 * 8) = v;
    }
  }
  __syncthreads();
  {
    const gu64* fp = (const gu64*)(ws + WS_F + (size_t)((unit * 4 + g) * 4) * 8);
    const int sl = lane & 3;
#pragma unroll 1
    for (unsigned spins = 0; spins < 20000000u; ++spins) {
      const unsigned long long v = __hip_atomic_load(fp + sl, __ATOMIC_RELAXED, __HIP_MEMORY_SCOPE_AGENT);
      const bool ok = (sl == g) || (v == tag_own);
      if (__all(ok)) break;
      __builtin_amdgcn_s_sleep(1);
    }
    __builtin_amdgcn_fence(__ATOMIC_ACQUIRE, "wavefront");
  }
  {
    f32x2 ml0 = *(lds_f32x2_p)(uintptr_t)(xml + r32 * 8);
    u32x4 ro[3][2];
    u32x2 rml[3];
#pragma unroll
    for (int i = 0; i < 3; ++i) {
      const int sg = (g + 1 + i) & 3;
      const int slab = (unit * 4 + g) * 4 + sg;
      rml[i] = __builtin_amdgcn_raw_buffer_load_b64(rsrc, (unsigned)(WS_ML + slab * 256) + r32 * 8, 0, 16);
#pragma unroll
      for (int hh = 0; hh < 2; ++hh)
        ro[i][hh] = __builtin_amdgcn_raw_buffer_load_b128(rsrc, (unsigned)(WS_O + (size_t)slab * 8192) + lane * 16 + (wid * 2 + hh) * 1024, 0, 16);
    }
    f32x4 own[4];
#pragma unroll
    for (int gq = 0; gq < 4; ++gq) own[gq] = *(lds_f32x4_p)(uintptr_t)(xo + (wid * 4 + gq) * 1024 + lane * 16);
    float M = ml0[0];
#pragma unroll
    for (int i = 0; i < 3; ++i) M = fmaxf(M, __uint_as_float(rml[i][0]));
    const float f0 = __builtin_amdgcn_exp2f(ml0[0] - M);
    float L = f0 * ml0[1];
    float f[3];
#pragma unroll
    for (int i = 0; i < 3; ++i) {
      f[i] = __builtin_amdgcn_exp2f(__uint_as_float(rml[i][0]) - M);
      L += f[i] * __uint_as_float(rml[i][1]);
    }
    const float inv = 1.f / L;
    char* orow = (char*)Op + ((size_t)(unit * 4 + g) * SQ + r32) * D * 2 + wid * 64 + h * 8;
#pragma unroll
    for (int gq = 0; gq < 4; ++gq) {
      float a[4];
#pragma unroll
      for (int e = 0; e < 4; ++e) {
        float x = f0 * own[gq][e];
#pragma unroll
        for (int i = 0; i < 3; ++i) {
          const unsigned wd = ro[i][gq >> 1][(gq & 1) * 2 + (e >> 1)];
          x += f[i] * __uint_as_float((e & 1) ? (wd & 0xffff0000u) : (wd << 16));
        }
        a[e] = x * inv;
      }
      u32x2 w = {pk_bf16(a[0], a[1]), pk_bf16(a[2], a[3])};
      *(u32x2*)(orow + gq * 16) = w;
    }
  }
  __syncthreads();
  if (tid == 128) *(unsigned long long*)(ws + WS_E + (unit * 4 + g) * 8) = tag_own;
}

extern "C" void kernel_launch(void* const* d_in, const int* in_sizes, int n_in, void* d_out, int out_size, void* d_ws, size_t ws_size,
                              hipStream_t stream) {
  (void)in_sizes; (void)n_in; (void)out_size; (void)ws_size;
  (void)hipFuncSetAttribute((const void*)gqa_kernel, hipFuncAttributeMaxDynamicSharedMemorySize, LDS_BYTES);
  hipLaunchKernelGGL(gqa_kernel, dim3(256), dim3(384), LDS_BYTES, stream, (const __hip_bfloat16*)d_in[0], (const __hip_bfloat16*)d_in[1],
                     (const __hip_bfloat16*)d_in[2], (__hip_bfloat16*)d_out, (char*)d_ws);
}
```

```cpp
#include <hip/hip_runtime.h>
#include <hip/hip_bf16.h>

typedef short bf16x8 __attribute__((ext_vector_type(8)));
typedef short s16x4 __attribute__((ext_vector_type(4)));
typedef float f32x16 __attribute__((ext_vector_type(16)));
typedef float f32x4 __attribute__((ext_vector_type(4)));
typedef float f32x2 __attribute__((ext_vector_type(2)));
typedef __bf16 bf16x2_t __attribute__((ext_vector_type(2)));
typedef unsigned u32x4 __attribute__((ext_vector_type(4)));
typedef unsigned u32x2 __attribute__((ext_vector_type(2)));
typedef __attribute__((address_space(3))) s16x4* lds_s16x4_p;
typedef __attribute__((address_space(3))) bf16x8* lds_bf16x8_p;
typedef __attribute__((address_space(3))) f32x4* lds_f32x4_p;
typedef __attribute__((address_space(3))) f32x2* lds_f32x2_p;
typedef __attribute__((address_space(1))) unsigned long long gu64;

constexpr int SQ = 32, SK = 2048, D = 128;
constexpr int NSPLIT = 4, KT = 32, KPW = SK / NSPLIT, NT = KPW / KT;
constexpr int NS = 8;
constexpr int LA = 4;
static_assert(LA <= NS - 3, "a stage is refilled three barriers after its tile landed");
constexpr int TILE_B = KT * D * 2;
constexpr int STAGE_B = 2 * TILE_B, RING_B = NS * STAGE_B;
constexpr int LDS_BYTES = RING_B;
constexpr float SC = 0.088388347648318447f * 1.4426950408889634f;
constexpr float THR = 8.f;
constexpr size_t WS_E = 0, WS_F = 32768, WS_ML = 262144, WS_O = 1 << 20;
constexpr size_t WS_END = WS_O + (size_t)64 * 4 * 4 * 8192;

__device__ __forceinline__ unsigned pk_bf16(float a, float b) {
  f32x2 v = {a, b};
  bf16x2_t r = __builtin_convertvector(v, bf16x2_t);
  return __builtin_bit_cast(unsigned, r);
}

__device__ __forceinline__ void dma8(const char* gbase, unsigned lds, unsigned o0, unsigned o1, unsigned o2, unsigned o3) {
  unsigned keep;
  const char* g2 = gbase + 4096;
  const unsigned lds2 = lds + 4096;
  asm volatile(
      "s_mov_b32 %0, m0\n\t"
      "s_mov_b32 m0, %7\n\t"
      "s_nop 0\n\t"
      "global_load_lds_dwordx4 %1, %5 offset:0\n\t"
      "global_load_lds_dwordx4 %2, %5 offset:1024\n\t"
      "global_load_lds_dwordx4 %3, %5 offset:2048\n\t"
      "global_load_lds_dwordx4 %4, %5 offset:3072\n\t"
      "s_mov_b32 m0, %8\n\t"
      "s_nop 0\n\t"
      "global_load_lds_dwordx4 %1, %6 offset:0\n\t"
      "global_load_lds_dwordx4 %2, %6 offset:1024\n\t"
      "global_load_lds_dwordx4 %3, %6 offset:2048\n\t"
      "global_load_lds_dwordx4 %4, %6 offset:3072\n\t"
      "s_mov_b32 m0, %0"
      : "=&s"(keep)
      : "v"(o0), "v"(o1), "v"(o2), "v"(o3), "s"(gbase), "s"(g2), "s"(lds), "s"(lds2)
      : "memory");
}

template <int N> __device__ __forceinline__ void wait_bar() { asm volatile("s_waitcnt vmcnt(%0)\n\ts_barrier" ::"i"(N) : "memory"); }
template <int I> __device__ __forceinline__ void tail_waits() {
  if constexpr (I >= 0) { wait_bar<8 * I>(); tail_waits<I - 1>(); }
}

__global__ __launch_bounds__(384, 2) void gqa_kernel(const __hip_bfloat16* __restrict__ Qp, const __hip_bfloat16* __restrict__ Kp,
                                                      const __hip_bfloat16* __restrict__ Vp, __hip_bfloat16* __restrict__ Op, char* __restrict__ ws) {
  extern __shared__ __attribute__((aligned(16))) char smem[];
  const int tid = threadIdx.x, lane = tid & 63;
  const int wv = __builtin_amdgcn_readfirstlane(tid >> 6);
  const int bid = blockIdx.x, xcd = bid & 7, idx = bid >> 3;
  const int unit = xcd * 8 + (idx >> 2), g = idx & 3;
  const unsigned smem_base = (unsigned)(uintptr_t)smem;

  if (wv < 2) {
    const int li = lane & 15, lq = lane >> 4;
    const unsigned sw = wv == 0 ? lq : (lq << 2);
    const unsigned o0 = lq * 256 + ((li ^ sw) * 16);
    const unsigned x = wv == 0 ? 64 : 0;
    const unsigned o1 = o0 ^ x, o2 = o0 ^ (2 * x), o3 = o0 ^ (3 * x);
    const char* src = (wv == 0 ? (const char*)Kp : (const char*)Vp) + ((size_t)unit * SK + (size_t)g * KPW) * D * 2;
    const unsigned dst0 = smem_base + wv * TILE_B;
    unsigned so = 0;
    if (wv == 0) {
#pragma unroll 1
      for (int t = 0; t < NT; ++t) {
        if (t >= LA) wait_bar<8 * (LA - 1)>();
        dma8(src, dst0 + so, o0, o1, o2, o3);
        src += TILE_B;
        so = (so + STAGE_B) & (RING_B - 1);
      }
      tail_waits<LA - 1>();
      asm volatile("s_barrier\n\ts_barrier" ::: "memory");
    } else {
#pragma unroll 1
      for (int t = 0; t < NT; ++t) {
        if (t >= LA) wait_bar<8 * LA>();
        dma8(src, dst0 + so, o0, o1, o2, o3);
        src += TILE_B;
        so = (so + STAGE_B) & (RING_B - 1);
      }
      tail_waits<LA>();
      asm volatile("s_barrier" ::: "memory");
    }
    return;
  }
  const int wid = wv - 2;
  const int r32 = lane & 31, h = lane >> 5;
  bf16x8 qf[8];
  {
    const char* Qb = (const char*)Qp + (size_t)(unit * 4 + wid) * SQ * D * 2 + r32 * 256 + h * 16;
#pragma unroll
    for (int ks = 0; ks < 8; ++ks) qf[ks] = *(const bf16x8*)(Qb + ks * 32);
  }
  const unsigned long long tag_pub = *(const unsigned long long*)(ws + WS_E + (unit * 4 + wid) * 128) + 1;
  const unsigned long long tag_own = *(const unsigned long long*)(ws + WS_E + (unit * 4 + g) * 128) + 1;

  const unsigned kr = smem_base + r32 * 256 + ((h ^ (r32 & 15)) * 16);
  const int tq = (lane & 15) >> 2, tp = lane & 3, tblk = (lane >> 4) & 1;
  const unsigned vr = smem_base + TILE_B + 256 * (4 * h + tq) + 32 * tblk + 8 * tp;

  f32x16 o[4];
#pragma unroll
  for (int i = 0; i < 4; ++i)
#pragma unroll
    for (int j = 0; j < 16; ++j) o[i][j] = 0.f;
  float m, l = 0.f;
  f32x16 sa, sb;
  const f32x16 zero = {};
  bf16x8 kf[8];
  s16x4 vlo[8], vhi[8];
  unsigned ka[8], va[4];
#pragma unroll
  for (int ks = 0; ks < 8; ++ks) ka[ks] = kr ^ (32 * ks);
#pragma unroll
  for (int db = 0; db < 4; ++db) va[db] = vr + 64 * (db ^ tq);
#define SB() __builtin_amdgcn_sched_barrier(0)
#define PIN(v) asm volatile("" : "+v"(v))
#define KREAD(ks, off) kf[ks] = *(lds_bf16x8_p)(uintptr_t)(ka[ks] + (off))
#define VREAD(i, off)                                                                                                          \
  do {                                                                                                                         \
    vlo[i] = __builtin_amdgcn_ds_read_tr16_b64_v4i16((lds_s16x4_p)(uintptr_t)(va[(i) & 3] + 4096 * ((i) >> 2) + (off)));        \
    vhi[i] = __builtin_amdgcn_ds_read_tr16_b64_v4i16((lds_s16x4_p)(uintptr_t)(va[(i) & 3] + 4096 * ((i) >> 2) + 2048 + (off))); \
  } while (0)
  {
    asm volatile("s_barrier" ::: "memory");
#pragma unroll
    for (int ks = 0; ks < 8; ++ks) KREAD(ks, 0);
    sa = __builtin_amdgcn_mfma_f32_32x32x16_bf16(kf[0], qf[0], zero, 0, 0, 0);
#pragma unroll
    for (int ks = 1; ks < 8; ++ks) sa = __builtin_amdgcn_mfma_f32_32x32x16_bf16(kf[ks], qf[ks], sa, 0, 0, 0);
    SB();
    asm volatile("s_barrier" ::: "memory");
#pragma unroll
    for (int ks = 0; ks < 8; ++ks) { KREAD(ks, STAGE_B); ka[ks] += 2 * STAGE_B; }
    float mx = sa[0];
#pragma unroll
    for (int j = 1; j < 16; ++j) mx = fmaxf(mx, sa[j]);
    auto rr = __builtin_amdgcn_permlane32_swap(__float_as_uint(mx), __float_as_uint(mx), false, false);
    m = fmaxf(__uint_as_float(rr[0]), __uint_as_float(rr[1])) * SC;
    SB();
  }
#define ITER(S, X, OFF, SECOND, LIVE)                                                                              \
  {                                                                                                                \
    unsigned pw[8];                                                                                                \
    _Pragma("unroll") for (int k = 0; k < 8; ++k) {                                                                \
      if (k == 0) S = __builtin_amdgcn_mfma_f32_32x32x16_bf16(kf[0], qf[0], zero, 0, 0, 0);                        \
      else S = __builtin_amdgcn_mfma_f32_32x32x16_bf16(kf[k], qf[k], S, 0, 0, 0);                                  \
      SB();                                                                                                        \
      VREAD(k, OFF);                                                                                               \
      X[2 * k] = __builtin_amdgcn_exp2f(fmaf(X[2 * k], SC, -m));                                                   \
      X[2 * k + 1] = __builtin_amdgcn_exp2f(fmaf(X[2 * k + 1], SC, -m));                                           \
      if (k > 0) {                                                                                                 \
        pw[k - 1] = pk_bf16(X[2 * k - 2], X[2 * k - 1]);                                                           \
        if (k & 1) { l += X[2 * k - 2]; PIN(l); }                                                                  \
      }                                                                                                            \
      SB();                                                                                                        \
    }                                                                                                              \
    pw[7] = pk_bf16(X[14], X[15]);                                                                                 \
    asm volatile("s_barrier" ::: "memory");                                                    \
    const bf16x8 pf0 = __builtin_bit_cast(bf16x8, u32x4{pw[0], pw[1], pw[2], pw[3]});                              \
    const bf16x8 pf1 = __builtin_bit_cast(bf16x8, u32x4{pw[4], pw[5], pw[6], pw[7]});                              \
    float mx = 0.f;                                                                                                \
    SB();                                                                                                          \
    _Pragma("unroll") for (int k = 0; k < 8; ++k) {                                                                \
      const bf16x8 vf = __builtin_shufflevector(vlo[k], vhi[k], 0, 1, 2, 3, 4, 5, 6, 7);                           \
      o[k & 3] = __builtin_amdgcn_mfma_f32_32x32x16_bf16(vf, k < 4 ? pf0 : pf1, o[k & 3], 0, 0, 0);                \
      SB();                                                                                                        \
      KREAD(k, OFF);                                                                                               \
      if (SECOND) { ka[k] += kstep; PIN(ka[k]); if (k < 4) { va[k] += vstep; PIN(va[k]); } }                       \
      l += X[2 * k + 1];                                                                                           \
      if (k & 1) l += X[2 * k];                                                                                    \
      PIN(l);                                                                                                      \
      if (k == 2) { PIN(S); mx = fmaxf(fmaxf(S[0], S[1]), S[2]); mx = fmaxf(fmaxf(mx, S[3]), S[4]); PIN(mx); }     \
      if (k == 3) { mx = fmaxf(fmaxf(mx, S[5]), S[6]); mx = fmaxf(fmaxf(mx, S[7]), S[8]); PIN(mx); }               \
      if (k == 4) { mx = fmaxf(fmaxf(mx, S[9]), S[10]); mx = fmaxf(fmaxf(mx, S[11]), S[12]); PIN(mx); }            \
      if (k == 5) { mx = fmaxf(fmaxf(mx, S[13]), S[14]); mx = fmaxf(mx, S[15]); PIN(mx); }                         \
      if (k == 6) {                                                                                                \
        auto rr = __builtin_amdgcn_permlane32_swap(__float_as_uint(mx), __float_as_uint(mx), false, false);        \
        mx = fmaf(fmaxf(__uint_as_float(rr[0]), __uint_as_float(rr[1])), SC, -m);                                  \
      }                                                                                                            \
      SB();                                                                                                        \
    }                                                                                                              \
                            \
    if (__builtin_expect((LIVE) && __any(!(mx <= THR)), 0)) {                                                      \
      const float d = fmaxf(mx, 0.f);                                                                              \
      const float alpha = __builtin_amdgcn_exp2f(-d);                                                              \
      m += d;                                                                                                      \
      l *= alpha;                                                                                                  \
      _Pragma("unroll") for (int i = 0; i < 4; ++i) _Pragma("unroll") for (int j = 0; j < 16; ++j) o[i][j] *= alpha; \
    }                                                                                                              \
    SB();                                                                                                          \
  }
#pragma unroll 1
  for (int j = 0; j < NT / 2; ++j) {
    const unsigned kstep = (j & 3) == 2 ? (unsigned)(2 * STAGE_B - RING_B) : (unsigned)(2 * STAGE_B);
    const unsigned vstep = (j & 3) == 3 ? (unsigned)(2 * STAGE_B - RING_B) : (unsigned)(2 * STAGE_B);
    ITER(sb, sa, 0, 0, true)
    ITER(sa, sb, STAGE_B, 1, j < NT / 2 - 1)
  }

  {
    auto rr = __builtin_amdgcn_permlane32_swap(__float_as_uint(l), __float_as_uint(l), false, false);
    l = __uint_as_float(rr[0]) + __uint_as_float(rr[1]);
  }
  const auto rsrc = __builtin_amdgcn_make_buffer_rsrc((void*)ws, 0, (int)WS_END, 0x00020000);
  const unsigned xo = smem_base + 2 * STAGE_B;
  const unsigned xml = smem_base + 3 * STAGE_B;
  if (wid != g) {
    const int slab = (unit * 4 + wid) * 4 + g;
    const unsigned ob = (unsigned)(WS_O + (size_t)slab * 8192) + lane * 16;
#pragma unroll
    for (int db = 0; db < 4; ++db)
#pragma unroll
      for (int hh = 0; hh < 2; ++hh) {
        u32x4 v = {pk_bf16(o[db][8 * hh], o[db][8 * hh + 1]), pk_bf16(o[db][8 * hh + 2], o[db][8 * hh + 3]), pk_bf16(o[db][8 * hh + 4], o[db][8 * hh + 5]),
                   pk_bf16(o[db][8 * hh + 6], o[db][8 * hh + 7])};
        __builtin_amdgcn_raw_buffer_store_b128(v, rsrc, ob + (db * 2 + hh) * 1024, 0, 16);
      }
    if (h == 0) {
      u32x2 v = {__float_as_uint(m), __float_as_uint(l)};
      __builtin_amdgcn_raw_buffer_store_b64(v, rsrc, (unsigned)(WS_ML + slab * 256) + r32 * 8, 0, 16);
    }
    asm volatile("s_waitcnt vmcnt(0)" ::: "memory");
    if (lane < 8) {
      const u32x4 fv = {(unsigned)tag_pub, (unsigned)(tag_pub >> 32), (unsigned)tag_pub, (unsigned)(tag_pub >> 32)};
      __builtin_amdgcn_raw_buffer_store_b128(fv, rsrc, (unsigned)(WS_F + slab * 128) + lane * 16, 0, 16);
    }
  } else {
#pragma unroll
    for (int db = 0; db < 4; ++db)
#pragma unroll
      for (int gq = 0; gq < 4; ++gq) {
        f32x4 v = {o[db][4 * gq], o[db][4 * gq + 1], o[db][4 * gq + 2], o[db][4 * gq + 3]};
        *(lds_f32x4_p)(uintptr_t)(xo + (db * 4 + gq) * 1024 + lane * 16) = v;
      }
    if (h == 0) {
      f32x2 v = {m, l};
      *(lds_f32x2_p)(uintptr_t)(xml + r32 * 8) = v;
    }
  }
  asm volatile("s_waitcnt lgkmcnt(0)\n\ts_barrier" ::: "memory");
  {
    const gu64* fp = (const gu64*)(ws + WS_F + (size_t)((unit * 4 + g) * 4) * 128);
    const int sl = lane & 3;
#pragma unroll 1
    for (unsigned spins = 0; spins < 100000000u; ++spins) {
      const unsigned long long v = __hip_atomic_load(fp + sl * 16, __ATOMIC_RELAXED, __HIP_MEMORY_SCOPE_AGENT);
      const bool ok = (sl == g) || (v == tag_own);
      if (__all(ok)) break;
      __builtin_amdgcn_s_sleep(1);
    }
    __builtin_amdgcn_fence(__ATOMIC_ACQUIRE, "wavefront");
  }
  {
    f32x2 ml0 = *(lds_f32x2_p)(uintptr_t)(xml + r32 * 8);
    u32x4 ro[3][2];
    u32x2 rml[3];
#pragma unroll
    for (int i = 0; i < 3; ++i) {
      const int sg = (g + 1 + i) & 3;
      const int slab = (unit * 4 + g) * 4 + sg;
      rml[i] = __builtin_amdgcn_raw_buffer_load_b64(rsrc, (unsigned)(WS_ML + slab * 256) + r32 * 8, 0, 16);
#pragma unroll
      for (int hh = 0; hh < 2; ++hh)
        ro[i][hh] = __builtin_amdgcn_raw_buffer_load_b128(rsrc, (unsigned)(WS_O + (size_t)slab * 8192) + lane * 16 + (wid * 2 + hh) * 1024, 0, 16);
    }
    f32x4 own[4];
#pragma unroll
    for (int gq = 0; gq < 4; ++gq) own[gq] = *(lds_f32x4_p)(uintptr_t)(xo + (wid * 4 + gq) * 1024 + lane * 16);
    float M = ml0[0];
#pragma unroll
    for (int i = 0; i < 3; ++i) M = fmaxf(M, __uint_as_float(rml[i][0]));
    const float f0 = __builtin_amdgcn_exp2f(ml0[0] - M);
    float L = f0 * ml0[1];
    float f[3];
#pragma unroll
    for (int i = 0; i < 3; ++i) {
      f[i] = __builtin_amdgcn_exp2f(__uint_as_float(rml[i][0]) - M);
      L += f[i] * __uint_as_float(rml[i][1]);
    }
    const float inv = 1.f / L;
    char* orow = (char*)Op + ((size_t)(unit * 4 + g) * SQ + r32) * D * 2 + wid * 64 + h * 8;
#pragma unroll
    for (int gq = 0; gq < 4; ++gq) {
      float a[4];
#pragma unroll
      for (int e = 0; e < 4; ++e) {
        float x = f0 * own[gq][e];
#pragma unroll
        for (int i = 0; i < 3; ++i) {
          const unsigned wd = ro[i][gq >> 1][(gq & 1) * 2 + (e >> 1)];
          x += f[i] * __uint_as_float((e & 1) ? (wd & 0xffff0000u) : (wd << 16));
        }
        a[e] = x * inv;
      }
      u32x2 w = {pk_bf16(a[0], a[1]), pk_bf16(a[2], a[3])};
      *(u32x2*)(orow + gq * 16) = w;
    }
  }
  __syncthreads();
  if (tid >= 128 && tid < 136) {
    const u32x4 ev = {(unsigned)tag_own, (unsigned)(tag_own >> 32), (unsigned)tag_own, (unsigned)(tag_own >> 32)};
    *(u32x4*)(ws + WS_E + (unit * 4 + g) * 128 + (tid - 128) * 16) = ev;
  }
}

extern "C" void kernel_launch(void* const* d_in, const int* in_sizes, int n_in, void* d_out, int out_size, void* d_ws, size_t ws_size,
                              hipStream_t stream) {
  (void)in_sizes; (void)n_in; (void)out_size; (void)ws_size;
  (void)hipFuncSetAttribute((const void*)gqa_kernel, hipFuncAttributeMaxDynamicSharedMemorySize, LDS_BYTES);
  hipLaunchKernelGGL(gqa_kernel, dim3(256), dim3(384), LDS_BYTES, stream, (const __hip_bfloat16*)d_in[0], (const __hip_bfloat16*)d_in[1],
                     (const __hip_bfloat16*)d_in[2], (__hip_bfloat16*)d_out, (char*)d_ws);
}
```
